```python
import torch

def _softmax(x, axis=-1):
    return torch.softmax(x, dim=axis)

def _gelu(x):
    return torch.nn.functional.gelu(x, approximate='tanh')

def _silu(x):
    return torch.nn.functional.silu(x)

def _sigmoid(x):
    return torch.sigmoid(x)

def _layernorm(x, axis=-1, eps=1e-05):
    m = x.mean(dim=axis, keepdim=True)
    v = x.var(dim=axis, keepdim=True, unbiased=False)
    return (x - m) / torch.sqrt(v + eps)

def _scatter_add(tgt, idx, src, axis=0):
    return tgt.clone().index_add_(axis, idx, src)

def _segment_sum(src, seg, n):
    out = torch.zeros((n,) + src.shape[1:], dtype=src.dtype)
    return out.index_add_(0, seg, src)

def _pad(x, pw):
    flat = tuple((v for pair in reversed(pw) for v in pair))
    return torch.nn.functional.pad(x, flat)
kernel_name = 'adaLN_cond__ssm_conv_h3__glu_gated_reglu__prompt'

def setup_inputs():

    _w = lambda a: (lambda t: t.to(torch.bfloat16) if t.is_floating_point() else t)(torch.as_tensor(a))

    def _A_setup():
        B, S, D = (1, 2048, 512)
        ks = 4
        return {'q': _w((lambda *s: torch.randn(*s, dtype=torch.bfloat16) * 0.05)(B, S, D)), 'k': _w((lambda *s: torch.randn(*s, dtype=torch.bfloat16) * 0.8)(B, S, D)), 'v': _w((lambda *s: torch.randn(*s, dtype=torch.bfloat16) * 0.8)(B, S, D)), 'shift_k': _w((lambda *s: torch.randn(*s, dtype=torch.bfloat16) * 0.8)(D, ks) * 0.5), 'ssm_k': _w((lambda *s: torch.randn(*s, dtype=torch.bfloat16) * 0.8)(D, S) * (1.0 / S ** 0.5)), 'ssm_D': _w((lambda *s: torch.randn(*s, dtype=torch.bfloat16) * 0.8)(D))}
    _A_in = _A_setup()

    def _B_setup():
        B, S, D, F = (1, 2048, 512, 1792)
        return {'x': _w((lambda *s: torch.randn(*s, dtype=torch.bfloat16) * 0.05)(B, S, D)), 'W_gate': _w((lambda *s: torch.randn(*s, dtype=torch.bfloat16) * 0.8)(D, F) * (2.0 / D) ** 0.5), 'W_up': _w((lambda *s: torch.randn(*s, dtype=torch.bfloat16) * 0.8)(D, F) * (2.0 / D) ** 0.5), 'W_down': _w((lambda *s: torch.randn(*s, dtype=torch.bfloat16) * 0.8)(F, D) * (1.0 / F) ** 0.5)}
    _B_in = _B_setup()
    return {'A_q': _A_in['q'], 'A_k': _A_in['k'], 'A_v': _A_in['v'], 'A_shift_k': _A_in['shift_k'], 'A_ssm_k': _A_in['ssm_k'], 'A_ssm_D': _A_in['ssm_D'], 'B_x': _B_in['x'], 'B_W_gate': _B_in['W_gate'], 'B_W_up': _B_in['W_up'], 'B_W_down': _B_in['W_down'], 'c': _w((lambda *s: torch.randn(*s, dtype=torch.bfloat16) * 0.2)(1, 512) * 8.0), 'w_mod': _w((lambda *s: torch.randn(*s, dtype=torch.bfloat16) * 0.2)(512, 6 * 512) * 512 ** (-0.5) * 8.0)}

def reference(A_q, A_k, A_v, A_shift_k, A_ssm_k, A_ssm_D, B_x, B_W_gate, B_W_up, B_W_down, c, w_mod):

    def _A_ref(q, k, v, shift_k, ssm_k, ssm_D):
        B, S, D = q.shape
        ks = shift_k.shape[-1]
        k_pad = _pad(k, ((0, 0), (ks - 1, 0), (0, 0)))
        k_shift = torch.zeros_like(k)
        for t in range(ks):
            k_shift = k_shift + k_pad[:, t:t + S, :] * shift_k[:, ks - 1 - t][None, None, :]
        kv = k_shift * v
        L = 2 * S
        kv_pad = _pad(kv, ((0, 0), (0, S), (0, 0)))
        kf_pad = _pad(ssm_k, ((0, 0), (0, S)))
        kv_f = (lambda x, axes=(-2, -1): torch.fft.rfft2(x.float(), dim=axes))(kv_pad[:, None, :, :], axes=(1, 2))
        kf_f = (lambda x, axes=(-2, -1): torch.fft.rfft2(x.float(), dim=axes))(kf_pad[:, None, :], axes=(1, 2))
        kf_f = (lambda x, *d: x.permute(*d))(kf_f, 1, 2, 0)
        y_f = kv_f * kf_f[None, :, :, :]
        y = (lambda x, s, axes=(-2, -1): torch.fft.irfft2(x, s=s, dim=axes).to(torch.bfloat16))(y_f, (1, L), axes=(1, 2))[:, 0, :S, :]
        y = y + kv * ssm_D[None, None, :]
        return q * y

    def _B_ref(x, W_gate, W_up, W_down):
        gate = torch.einsum('bsd,df->bsf', x, W_gate)
        up = torch.einsum('bsd,df->bsf', x, W_up)
        gate = gate * (lambda x, dt: x.to(dt))(gate > 0, gate.dtype)
        h = gate * up
        return torch.einsum('bsf,fd->bsd', h, W_down)
    x = A_q
    mod = (_silu(c) @ w_mod).reshape(c.shape[0], 6, 1, -1)
    sa, ca, ga, sb, cb, gb = (mod[:, 0].clone(), mod[:, 1].clone(), mod[:, 2].clone(), mod[:, 3].clone(), mod[:, 4].clone(), mod[:, 5].clone())
    h = _layernorm(x, axis=-1) * (1 + ca) + sa
    x = x + ga * _A_ref(h, A_k, A_v, A_shift_k, A_ssm_k, A_ssm_D)
    h = _layernorm(x, axis=-1) * (1 + cb) + sb
    x = x + gb * _B_ref(h, B_W_gate, B_W_up, B_W_down)
    return x
```

```cpp
#include <hip/hip_runtime.h>
#include <hip/hip_bf16.h>
#include <stdint.h>

typedef __attribute__((ext_vector_type(2))) float f32x2;
typedef __attribute__((ext_vector_type(4))) float f32x4;
typedef __attribute__((ext_vector_type(2))) __bf16 bf16x2;
typedef __attribute__((ext_vector_type(8))) __bf16 bf16x8;
typedef __attribute__((ext_vector_type(4))) unsigned u32x4;
typedef __attribute__((ext_vector_type(2))) unsigned u32x2;
typedef unsigned short u16;

#define S_LEN 2048
#define D_DIM 512
#define F_DIM 1792

#define WS_KVT   0u
#define WS_STAT1 (WS_KVT + 2097152u)
#define WS_MODP  (WS_STAT1 + 16384u)
#define WS_CSB   (WS_MODP + 49152u)
#define WS_X1F   (WS_CSB + 114688u)
#define WS_W1F   (WS_X1F + 2097152u)
#define WS_WDF   (WS_W1F + 3670016u)
#define WS_ACTF  (WS_WDF + 1835008u)

__device__ __forceinline__ unsigned pack2(float a, float b) {
  f32x2 v = {a, b};
  bf16x2 r = __builtin_convertvector(v, bf16x2);
  return __builtin_bit_cast(unsigned, r);
}
__device__ __forceinline__ float rbf(float a) { return (float)(__bf16)a; }
__device__ __forceinline__ float blo(unsigned w) { return __builtin_bit_cast(float, w << 16); }
__device__ __forceinline__ float bhi(unsigned w) { return __builtin_bit_cast(float, w & 0xffff0000u); }
__device__ __forceinline__ float bf2f(u16 h) { return __builtin_bit_cast(float, (unsigned)h << 16); }

__device__ __forceinline__ float wave_sum(float v) {
#pragma unroll
  for (int o = 32; o >= 1; o >>= 1) v += __shfl_xor(v, o, 64);
  return v;
}

#define MFMA16(a, b, c) __builtin_amdgcn_mfma_f32_16x16x32_bf16(__builtin_bit_cast(bf16x8, a), __builtin_bit_cast(bf16x8, b), c, 0, 0, 0)

__global__ __launch_bounds__(256) void k0_prep(const u16* __restrict__ q, const u16* __restrict__ k,
                                               const u16* __restrict__ v, const u16* __restrict__ shift_k,
                                               const u16* __restrict__ cvec, const u16* __restrict__ w_mod,
                                               char* __restrict__ ws) {
  __shared__ __attribute__((aligned(16))) unsigned sm[64 * 34 + 4 * 64];
  const int t = threadIdx.x, b = blockIdx.x;
  const int lane = t & 63, wv = t >> 6;
  u16* kvt = (u16*)(ws + WS_KVT);

  const int dt = b & 7, st = b >> 3;
  const int o = t & 7, rg = t >> 3;
  const int r0 = 64 * st + 2 * rg;
  const int dbase = 64 * dt + 8 * o;
  u32x4 kr[5], vr[2], wk[4];
#pragma unroll
  for (int i = 0; i < 5; ++i) {
    int r = r0 - 3 + i;
    u32x4 z = {0, 0, 0, 0};
    kr[i] = (r >= 0) ? *(const u32x4*)(k + (size_t)r * D_DIM + dbase) : z;
  }
#pragma unroll
  for (int i = 0; i < 2; ++i) vr[i] = *(const u32x4*)(v + (size_t)(r0 + i) * D_DIM + dbase);
#pragma unroll
  for (int i = 0; i < 4; ++i) wk[i] = *(const u32x4*)(shift_k + (size_t)dbase * 4 + i * 8);

  u32x4 qr[2];
#pragma unroll
  for (int i = 0; i < 2; ++i) qr[i] = *(const u32x4*)(q + (size_t)(8 * b + 2 * wv + i) * D_DIM + lane * 8);

  const bool doC = b < 192;
  const int strip = b % 48, dq = b / 48;
  u32x4 wm[4];
  float sc[4];
  if (doC) {
#pragma unroll
    for (int i = 0; i < 4; ++i) {
      int row = 128 * dq + rg + 32 * i;
      wm[i] = *(const u32x4*)(w_mod + (size_t)row * 3072 + 64 * strip + 8 * o);
      float c = bf2f(cvec[row]);
      sc[i] = rbf(c / (1.0f + __expf(-c)));
    }
  }

#pragma unroll
  for (int i = 0; i < 8; ++i) {
    unsigned wa = wk[i >> 1][(i & 1) * 2], wb = wk[i >> 1][(i & 1) * 2 + 1];
    float w0 = blo(wa), w1 = bhi(wa), w2 = blo(wb), w3 = bhi(wb);
    float kk[5];
#pragma unroll
    for (int r = 0; r < 5; ++r) kk[r] = (i & 1) ? bhi(kr[r][i >> 1]) : blo(kr[r][i >> 1]);
    float res[2];
#pragma unroll
    for (int r = 0; r < 2; ++r) {
      float a = rbf(kk[r] * w3);
      a = rbf(a + rbf(kk[r + 1] * w2));
      a = rbf(a + rbf(kk[r + 2] * w1));
      a = rbf(a + rbf(kk[r + 3] * w0));
      float vv = (i & 1) ? bhi(vr[r][i >> 1]) : blo(vr[r][i >> 1]);
      res[r] = a * vv;
    }
    sm[(8 * o + i) * 34 + (31 - rg)] = pack2(res[1], res[0]);
  }

#pragma unroll
  for (int i = 0; i < 2; ++i) {
    float x[8];
#pragma unroll
    for (int j = 0; j < 4; ++j) { x[2 * j] = blo(qr[i][j]); x[2 * j + 1] = bhi(qr[i][j]); }
    float s = 0.f;
#pragma unroll
    for (int j = 0; j < 8; ++j) s += x[j];
    s = wave_sum(s);
    float mean = s * (1.0f / D_DIM);
    float ss = 0.f;
#pragma unroll
    for (int j = 0; j < 8; ++j) { float d = x[j] - mean; ss += d * d; }
    ss = wave_sum(ss);
    float var = rbf(ss * (1.0f / D_DIM));
    float sd = rbf(sqrtf(rbf(var + 1e-5f)));
    if (lane == 0) {
      f32x2 o2 = {rbf(mean), 1.0f / sd};
      *(f32x2*)(ws + WS_STAT1 + (size_t)(8 * b + 2 * wv + i) * 8) = o2;
    }
  }

  float* smc = (float*)(sm + 64 * 34);
  if (doC) {
    float acc[8];
#pragma unroll
    for (int j = 0; j < 8; ++j) acc[j] = 0.f;
#pragma unroll
    for (int i = 0; i < 4; ++i)
#pragma unroll
      for (int j = 0; j < 4; ++j) {
        acc[2 * j] += sc[i] * blo(wm[i][j]);
        acc[2 * j + 1] += sc[i] * bhi(wm[i][j]);
      }
#pragma unroll
    for (int j = 0; j < 8; ++j) {
      acc[j] += __shfl_xor(acc[j], 8, 64);
      acc[j] += __shfl_xor(acc[j], 16, 64);
      acc[j] += __shfl_xor(acc[j], 32, 64);
    }
    if (lane < 8) {
#pragma unroll
      for (int j = 0; j < 8; ++j) smc[wv * 64 + lane * 8 + j] = acc[j];
    }
  }
  __syncthreads();
  {
    const int ch = t >> 2, seg = t & 3;
    const u32x2* p = (const u32x2*)(sm + ch * 34 + seg * 8);
    u32x2 a0 = p[0], a1 = p[1], a2 = p[2], a3 = p[3];
    u32x4 o0 = {a0[0], a0[1], a1[0], a1[1]}, o1 = {a2[0], a2[1], a3[0], a3[1]};
    u16* dst = kvt + (size_t)(64 * dt + ch) * S_LEN + (S_LEN - 64 * (st + 1)) + seg * 16;
    *(u32x4*)dst = o0;
    *(u32x4*)(dst + 8) = o1;
  }
  if (doC && t < 64) {
    float s = smc[t] + smc[64 + t] + smc[128 + t] + smc[192 + t];
    ((float*)(ws + WS_MODP))[dq * 3072 + 64 * strip + t] = s;
  }
}

__device__ __forceinline__ float mod_val(const char* ws, int idx) {
  const float* p = (const float*)(ws + WS_MODP);
  return rbf((p[idx] + p[3072 + idx]) + (p[6144 + idx] + p[9216 + idx]));
}

#define HC_STRIDE 272
#define HC_BYTES (8 * HC_STRIDE * 16 + 256)
#define UT_ROWS 48
#define UT_BYTES (8 * UT_ROWS * 16)
#define K1_OFF_H 0
#define K1_OFF_U (2 * HC_BYTES)
#define K1_OFF_M (K1_OFF_U + 2 * UT_BYTES)
#define K1_OFF_R (K1_OFF_M + 1024)
#define K1_LDS (K1_OFF_R + 8 * 32 * 16)
#define Y_ROW 272
#define Y_WAVE (32 * Y_ROW)

template <int KS0, int KS1>
__device__ __forceinline__ void conv_wave(const char* hbase, const char* ubase, f32x4 (&acc)[2][4]) {
  u32x4 F[4];
  constexpr int P0 = 2 * KS0;
  if (P0 >= 2) { F[0] = *(const u32x4*)(hbase + (P0 + 0) * 32); F[1] = *(const u32x4*)(hbase + (P0 + 1) * 32); }
#pragma unroll
  for (int ks = KS0; ks < KS1; ++ks) {
    const int p0 = 2 * ks;
    F[2] = *(const u32x4*)(hbase + (p0 + 2) * 32);
    F[3] = *(const u32x4*)(hbase + (p0 + 3) * 32);
    const int kap = ks >> 1, e = ks & 1;
    u32x4 B1 = *(const u32x4*)(ubase + (4 * e * UT_ROWS + kap - 16) * 16);
    if (ks < 32) {
      u32x4 B0 = *(const u32x4*)(ubase + (4 * e * UT_ROWS + kap) * 16);
#pragma unroll
      for (int mt = 0; mt < 4; ++mt)
        if (p0 + mt >= 2) acc[0][mt] = MFMA16(F[mt], B0, acc[0][mt]);
    }
#pragma unroll
    for (int mt = 0; mt < 4; ++mt)
      if (p0 + mt >= 2) acc[1][mt] = MFMA16(F[mt], B1, acc[1][mt]);
    F[0] = F[2]; F[1] = F[3];
  }
}

__global__ __launch_bounds__(512) void k1_conv(const u16* __restrict__ q, const u16* __restrict__ ssm_k,
                                               const u16* __restrict__ ssm_D, const u16* __restrict__ Wg,
                                               const u16* __restrict__ Wu, const u16* __restrict__ Wd,
                                               char* __restrict__ ws) {
  extern __shared__ __attribute__((aligned(16))) char smem[];
  const int t = threadIdx.x, b = blockIdx.x;
  const int lane = t & 63, wv = t >> 6;
  const int d0 = 64 * (b & 7) + 2 * (b >> 3);
  const int ch = t >> 8, tau = t & 255;

  const u16* hrow = ssm_k + (size_t)(d0 + ch) * S_LEN;
  const int Q = tau - 1;
  u32x4 zero4 = {0, 0, 0, 0};
  u32x4 h0 = (Q >= 0) ? *(const u32x4*)(hrow + 8 * Q) : zero4;
  u32x4 h1 = *(const u32x4*)(hrow + 8 * (Q + 1));
  const int uP = (tau & ~63) + (lane & 7) * 8 + (lane >> 3);
  u32x4 uu = *(const u32x4*)((const u16*)(ws + WS_KVT) + (size_t)(d0 + ch) * S_LEN + 8 * uP);

  unsigned xq[4];
#pragma unroll
  for (int i = 0; i < 4; ++i) xq[i] = *(const unsigned*)(q + (size_t)(4 * t + i) * D_DIM + d0);
  f32x4 st0 = *(const f32x4*)(ws + WS_STAT1 + (size_t)t * 32);
  f32x4 st1 = *(const f32x4*)(ws + WS_STAT1 + (size_t)t * 32 + 16);

  const bool side = b < 224;
  const int strip = b >> 2, dq = b & 3;
  const int fp = t & 31, doct = t >> 5;
  unsigned w1[8], wd[8];
  if (side) {
    const u16* W = (strip < 28) ? Wg : Wu;
    const int f0 = 64 * (strip % 28) + 2 * fp;
#pragma unroll
    for (int i = 0; i < 8; ++i) w1[i] = *(const unsigned*)(W + (size_t)(128 * dq + 8 * doct + i) * F_DIM + f0);
    if (t < 256) {
#pragma unroll
      for (int i = 0; i < 8; ++i) wd[i] = *(const unsigned*)(Wd + (size_t)(8 * b + i) * D_DIM + 2 * t);
    }
  }
  float* smod = (float*)(smem + K1_OFF_M);
  if (t < 256) {
    int which = t >> 7, dd = 128 * dq + (t & 127);
    float m = mod_val(ws, (which ? 3 : 4) * 512 + dd);
    smod[t] = which ? m : rbf(1.0f + m);
  }
  float sa[2], c1[2], ga[2], Dd[2];
#pragma unroll
  for (int i = 0; i < 2; ++i) {
    sa[i] = mod_val(ws, 0 * 512 + d0 + i);
    c1[i] = rbf(1.0f + mod_val(ws, 1 * 512 + d0 + i));
    ga[i] = mod_val(ws, 2 * 512 + d0 + i);
    Dd[i] = bf2f(ssm_D[d0 + i]);
  }

  {
    char* hb = smem + K1_OFF_H + ch * HC_BYTES;
    unsigned w[8] = {h0[0], h0[1], h0[2], h0[3], h1[0], h1[1], h1[2], h1[3]};
    unsigned od[7];
#pragma unroll
    for (int i = 0; i < 7; ++i) od[i] = __builtin_amdgcn_alignbit(w[i + 1], w[i], 16);
#pragma unroll
    for (int c = 0; c < 8; ++c) {
      const int cp = c + 1, e = cp >> 1;
      const int beta = (c & 1) + 4 * ((c >> 1) & 1) + 8 * (c >> 2);
      u32x4 val;
      if (cp & 1) val = (u32x4){od[e], od[e + 1], od[e + 2], od[e + 3]};
      else val = (u32x4){w[e], w[e + 1], w[e + 2], w[e + 3]};
      *(u32x4*)(hb + (c * HC_STRIDE + beta + Q + 8) * 16) = val;
    }
    if (tau < 24) {
      const int c = tau / 3, pc = 4 + tau % 3;
      const int beta = (c & 1) + 4 * ((c >> 1) & 1) + 8 * (c >> 2);
      *(u32x4*)(hb + (c * HC_STRIDE + beta + pc) * 16) = zero4;
    }
    char* ub = smem + K1_OFF_U + ch * UT_BYTES;
    *(u32x4*)(ub + ((uP & 7) * UT_ROWS + (uP >> 3)) * 16) = uu;
    if (tau < 128) *(u32x4*)(ub + ((tau >> 4) * UT_ROWS + 32 + (tau & 15)) * 16) = zero4;
  }
  __syncthreads();

  f32x4 acc[2][4];
#pragma unroll
  for (int a = 0; a < 2; ++a)
#pragma unroll
    for (int m = 0; m < 4; ++m) acc[a][m] = (f32x4){0.f, 0.f, 0.f, 0.f};
  const int wch = wv >> 2, wk = wv & 3;
  {
    const int i = lane & 15, g = lane >> 4;
    const int c = i & 7;
    const int beta = (c & 1) + 4 * ((c >> 1) & 1) + 8 * (c >> 2);
    const char* hbase = smem + K1_OFF_H + wch * HC_BYTES + (c * HC_STRIDE + beta + g + (i >> 3)) * 16;
    const char* ubase = smem + K1_OFF_U + wch * UT_BYTES + (g * UT_ROWS + 31 - i) * 16;
    const int wku = __builtin_amdgcn_readfirstlane(wk);
    if (wku == 0) conv_wave<0, 12>(hbase, ubase, acc);
    else if (wku == 1) conv_wave<12, 24>(hbase, ubase, acc);
    else if (wku == 2) conv_wave<24, 40>(hbase, ubase, acc);
    else conv_wave<40, 64>(hbase, ubase, acc);
  }

  if (side) {
    const int is_up = strip >= 28;
    const int f = 64 * (strip % 28) + 2 * fp;
    float lo[8], hi[8];
    float cs0 = 0.f, cs1 = 0.f, bb0 = 0.f, bb1 = 0.f;
#pragma unroll
    for (int i = 0; i < 8; ++i) {
      float c1b = smod[8 * doct + i], sbv = smod[128 + 8 * doct + i];
      float a0 = blo(w1[i]), a1 = bhi(w1[i]);
      lo[i] = rbf(a0 * c1b); hi[i] = rbf(a1 * c1b);
      cs0 += lo[i]; cs1 += hi[i];
      bb0 += sbv * a0; bb1 += sbv * a1;
    }
    u32x4 p0 = {pack2(lo[0], lo[1]), pack2(lo[2], lo[3]), pack2(lo[4], lo[5]), pack2(lo[6], lo[7])};
    u32x4 p1 = {pack2(hi[0], hi[1]), pack2(hi[2], hi[3]), pack2(hi[4], hi[5]), pack2(hi[6], hi[7])};
    const int d = 128 * dq + 8 * doct;
    const int kb = d >> 5, g = (d & 31) >> 3;
    const int P = f >> 4, gp = (f & 15) >> 2, hh = (f >> 1) & 1;
    const int mt = 2 * P + hh, r = 4 * gp + 2 * is_up;
    char* dst = ws + WS_W1F + ((size_t)(kb * 224 + mt) * 64 + g * 16 + r) * 16;
    *(u32x4*)dst = p0;
    *(u32x4*)(dst + 16) = p1;
    cs0 += __shfl_xor(cs0, 32, 64); cs1 += __shfl_xor(cs1, 32, 64);
    bb0 += __shfl_xor(bb0, 32, 64); bb1 += __shfl_xor(bb1, 32, 64);
    if (lane < 32) *(f32x4*)(smem + K1_OFF_R + (wv * 32 + fp) * 16) = (f32x4){cs0, bb0, cs1, bb1};
    if (t < 256) {
      u32x4 q0, q1;
#pragma unroll
      for (int i = 0; i < 4; ++i) {
        q0[i] = __builtin_amdgcn_perm(wd[2 * i + 1], wd[2 * i], 0x05040100u);
        q1[i] = __builtin_amdgcn_perm(wd[2 * i + 1], wd[2 * i], 0x07060302u);
      }
      const int dd = 2 * t;
      const int kbf = b >> 2, gf = b & 3;
      char* dw = ws + WS_WDF + ((size_t)(kbf * 32 + (dd >> 4)) * 64 + gf * 16 + (dd & 15)) * 16;
      *(u32x4*)dw = q0;
      *(u32x4*)(dw + 16) = q1;
    }
  }

  __syncthreads();
  {
    char* yb = smem + (wch * 4 + wk) * Y_WAVE;
    const int c = lane & 15, gp = lane >> 4;
#pragma unroll
    for (int nt = 0; nt < 2; ++nt)
#pragma unroll
      for (int mt = 0; mt < 4; ++mt)
        *(f32x4*)(yb + (16 * nt + c) * Y_ROW + (16 * mt + 4 * gp) * 4) = acc[nt][mt];
  }
  __syncthreads();
  if (side && t < 32) {
    f32x4 s = {0.f, 0.f, 0.f, 0.f};
#pragma unroll
    for (int w = 0; w < 8; ++w) s += *(const f32x4*)(smem + K1_OFF_R + (w * 32 + t) * 16);
    const int is_up = strip >= 28;
    const int f = 64 * (strip % 28) + 2 * t;
    const int P = f >> 4, gp = (f & 15) >> 2, hh = (f >> 1) & 1;
    const int R = (2 * P + hh) * 16 + 4 * gp + 2 * is_up;
    *(f32x4*)(ws + WS_CSB + ((size_t)dq * 3584 + R) * 8) = s;
  }
  {
    const int n = t >> 4, off = (t & 15) * 16;
    float res[2][4];
    float mean[4] = {st0[0], st0[2], st1[0], st1[2]};
    float rstd[4] = {st0[1], st0[3], st1[1], st1[3]};
#pragma unroll
    for (int c2 = 0; c2 < 2; ++c2) {
      f32x4 y = {0.f, 0.f, 0.f, 0.f};
#pragma unroll
      for (int w = 0; w < 4; ++w) y += *(const f32x4*)(smem + (c2 * 4 + w) * Y_WAVE + n * Y_ROW + off);
      const int p0 = 2044 - 4 * t;
      const int P = p0 >> 3;
      u32x2 kvw = *(const u32x2*)(smem + K1_OFF_U + c2 * UT_BYTES + ((P & 7) * UT_ROWS + (P >> 3)) * 16 + (p0 & 7) * 2);
      float kv[4] = {bhi(kvw[1]), blo(kvw[1]), bhi(kvw[0]), blo(kvw[0])};
#pragma unroll
      for (int i = 0; i < 4; ++i) {
        float x = c2 ? bhi(xq[i]) : blo(xq[i]);
        float n1 = rbf(rbf(x - mean[i]) * rstd[i]);
        float h1v = rbf(rbf(n1 * c1[c2]) + sa[c2]);
        float z = rbf(rbf(y[i]) + rbf(kv[i] * Dd[c2]));
        float a = rbf(h1v * z);
        res[c2][i] = x + rbf(ga[c2] * a);
      }
    }
    const int kb = d0 >> 5, g = (d0 & 31) >> 3, j = d0 & 7;
    const int B = t >> 4, cx = t & 15;
#pragma unroll
    for (int i = 0; i < 4; ++i) {
      char* dst = ws + WS_X1F + ((size_t)(kb * 128 + 4 * B + i) * 64 + g * 16 + cx) * 16 + j * 2;
      *(unsigned*)dst = pack2(res[0][i], res[1][i]);
    }
  }
}

#define G1_STAGE (22 * 1024)
#define G1_NST 7
#define G1_OFF_X (G1_NST * G1_STAGE)
#define G1_LDS (G1_OFF_X + 1792 + 1024)

#define GLDS16(gp, lp) __builtin_amdgcn_global_load_lds((const __attribute__((address_space(1))) void*)(gp), (__attribute__((address_space(3))) void*)(lp), 16, 0, 0)

__device__ __forceinline__ void g1_issue(const char* xsrc, const char* wsrc, char* slot, int kb, int w, int lane) {
  const char* xs = xsrc + (size_t)kb * (128 * 1024) + lane * 16;
  const char* wsr = wsrc + (size_t)kb * (224 * 1024) + lane * 16;
  GLDS16(xs + w * 1024, slot + w * 1024);
  GLDS16(xs + (w + 4) * 1024, slot + (w + 4) * 1024);
  GLDS16(wsr + w * 1024, slot + (8 + w) * 1024);
  GLDS16(wsr + (w + 4) * 1024, slot + (12 + w) * 1024);
  GLDS16(wsr + (w + 8) * 1024, slot + (16 + w) * 1024);
  if (w < 2) GLDS16(wsr + (w + 12) * 1024, slot + (20 + w) * 1024);
}

template <int N>
__device__ __forceinline__ void wait_stages(int w) {
  if (w < 2) asm volatile("s_waitcnt vmcnt(%0)" ::"i"(6 * N) : "memory");
  else asm volatile("s_waitcnt vmcnt(%0)" ::"i"(5 * N) : "memory");
}

__global__ __launch_bounds__(256) void k2_gemm1(char* __restrict__ ws) {
  extern __shared__ __attribute__((aligned(16))) char smem[];
  const int t = threadIdx.x, b = blockIdx.x;
  const int lane = t & 63;
  const int w = __builtin_amdgcn_readfirstlane(t >> 6);
  const int xcd = b & 7, idx = b >> 3;
  const int rt = 4 * (xcd >> 1) + (idx >> 3), ct = 8 * (xcd & 1) + (idx & 7);
  const int ws_ = w >> 1, wf = w & 1;

  const char* xsrc = ws + WS_X1F + (size_t)(8 * rt) * 1024;
  const char* wsrc = ws + WS_W1F + (size_t)(14 * ct) * 1024;

  if (t < 224) {
    const f32x2* p = (const f32x2*)(ws + WS_CSB) + 224 * ct + t;
    f32x2 s = (p[0] + p[3584]) + (p[2 * 3584] + p[3 * 3584]);
    *(f32x2*)(smem + G1_OFF_X + t * 8) = s;
  }
  asm volatile("s_waitcnt vmcnt(0)" ::: "memory");

#pragma unroll
  for (int s = 0; s < G1_NST; ++s) g1_issue(xsrc, wsrc, smem + s * G1_STAGE, s, w, lane);

  f32x4 acc[7][4];
#pragma unroll
  for (int m = 0; m < 7; ++m)
#pragma unroll
    for (int n = 0; n < 4; ++n) acc[m][n] = (f32x4){0.f, 0.f, 0.f, 0.f};
  f32x4 gram[2], rsum[2];
#pragma unroll
  for (int n = 0; n < 2; ++n) { gram[n] = (f32x4){0.f, 0.f, 0.f, 0.f}; rsum[n] = (f32x4){0.f, 0.f, 0.f, 0.f}; }
  const u32x4 ones = {0x3f803f80u, 0x3f803f80u, 0x3f803f80u, 0x3f803f80u};

  const char* xl = smem + (4 * ws_) * 1024 + lane * 16;
  const char* wl = smem + (8 + 7 * wf) * 1024 + lane * 16;

  u32x4 xf[2][4], wfr[2][7];
  wait_stages<G1_NST - 1>(w);
  __builtin_amdgcn_s_barrier();
#pragma unroll
  for (int n = 0; n < 4; ++n) xf[0][n] = *(const u32x4*)(xl + n * 1024);
#pragma unroll
  for (int m = 0; m < 7; ++m) wfr[0][m] = *(const u32x4*)(wl + m * 1024);

#pragma unroll
  for (int i = 0; i < 16; ++i) {
    const int cur = i & 1, nxt = cur ^ 1;
    if (i + 1 < 16) {
      constexpr int dummy = 0; (void)dummy;
      const int issued = (i + G1_NST < 16) ? (i + G1_NST) : 16;
      const int inflight = issued - (i + 2);
      switch (inflight) {
        case 5: wait_stages<5>(w); break;
        case 4: wait_stages<4>(w); break;
        case 3: wait_stages<3>(w); break;
        case 2: wait_stages<2>(w); break;
        case 1: wait_stages<1>(w); break;
        default: wait_stages<0>(w); break;
      }
      asm volatile("s_waitcnt lgkmcnt(0)" ::: "memory");
      __builtin_amdgcn_s_barrier();
      if (i + G1_NST < 16) g1_issue(xsrc, wsrc, smem + (i % G1_NST) * G1_STAGE, i + G1_NST, w, lane);
      const int so = ((i + 1) % G1_NST) * G1_STAGE;
#pragma unroll
      for (int n = 0; n < 4; ++n) xf[nxt][n] = *(const u32x4*)(xl + so + n * 1024);
#pragma unroll
      for (int m = 0; m < 7; ++m) wfr[nxt][m] = *(const u32x4*)(wl + so + m * 1024);
    }
#pragma unroll
    for (int m = 0; m < 7; ++m)
#pragma unroll
      for (int n = 0; n < 4; ++n) acc[m][n] = MFMA16(wfr[cur][m], xf[cur][n], acc[m][n]);
    if (wf == 0) {
#pragma unroll
      for (int n = 0; n < 2; ++n) { gram[n] = MFMA16(xf[cur][n], xf[cur][n], gram[n]); rsum[n] = MFMA16(ones, xf[cur][n], rsum[n]); }
    } else {
#pragma unroll
      for (int n = 0; n < 2; ++n) { gram[n] = MFMA16(xf[cur][n + 2], xf[cur][n + 2], gram[n]); rsum[n] = MFMA16(ones, xf[cur][n + 2], rsum[n]); }
    }
  }

  f32x2* sst = (f32x2*)(smem + G1_OFF_X + 1792);
  {
    const int c = lane & 15, gp = lane >> 4;
#pragma unroll
    for (int n = 0; n < 2; ++n) {
      if (gp == (c >> 2)) {
        float ss = (c & 3) == 0 ? gram[n][0] : (c & 3) == 1 ? gram[n][1] : (c & 3) == 2 ? gram[n][2] : gram[n][3];
        float mean = rsum[n][0] * (1.0f / D_DIM);
        float var = ss * (1.0f / D_DIM) - mean * mean;
        var = rbf(fmaxf(var, 0.f));
        float sd = rbf(sqrtf(rbf(var + 1e-5f)));
        sst[(4 * ws_ + 2 * wf + n) * 16 + c] = (f32x2){rbf(mean), 1.0f / sd};
      }
    }
  }
  __syncthreads();
  {
    const int c = lane & 15, gp = lane >> 4;
    f32x2 stt[4];
#pragma unroll
    for (int n = 0; n < 4; ++n) stt[n] = sst[(4 * ws_ + n) * 16 + c];
    char* actf = ws + WS_ACTF;
#pragma unroll
    for (int m = 0; m < 7; ++m) {
      const int ml = 7 * wf + m;
      const f32x4* cp = (const f32x4*)(smem + G1_OFF_X + (ml * 16 + 4 * gp) * 8);
      f32x4 cb0 = cp[0], cb1 = cp[1];
      const int mt = 14 * ct + ml;
      const int P = mt >> 1, hh = mt & 1;
      const int f = 16 * P + 4 * gp + 2 * hh;
      const int kb = f >> 5, g = (f & 31) >> 3, j = f & 7;
#pragma unroll
      for (int n = 0; n < 4; ++n) {
        const float mean = stt[n][0], rstd = stt[n][1];
        float g0 = rbf(rstd * (acc[m][n][0] - mean * cb0[0]) + cb0[1]);
        float g1 = rbf(rstd * (acc[m][n][1] - mean * cb0[2]) + cb0[3]);
        float u0 = rbf(rstd * (acc[m][n][2] - mean * cb1[0]) + cb1[1]);
        float u1 = rbf(rstd * (acc[m][n][3] - mean * cb1[2]) + cb1[3]);
        g0 = g0 > 0.f ? g0 : 0.f;
        g1 = g1 > 0.f ? g1 : 0.f;
        const int T = 8 * rt + 4 * ws_ + n;
        *(unsigned*)(actf + ((size_t)(kb * 128 + T) * 64 + g * 16 + c) * 16 + j * 2) = pack2(g0 * u0, g1 * u1);
      }
    }
  }
}

#define G2_ROW 272
#define G2_WAVE (64 * G2_ROW)
#define G2_LDS (4 * G2_WAVE)

__global__ __launch_bounds__(256) void k3_gemm2(const char* __restrict__ ws, u16* __restrict__ out) {
  extern __shared__ __attribute__((aligned(16))) char smem[];
  const int t = threadIdx.x, b = blockIdx.x;
  const int lane = t & 63;
  const int w = __builtin_amdgcn_readfirstlane(t >> 6);
  const int xcd = b & 7, idx = b >> 3;
  const int B = 4 * xcd + (idx >> 3), dtile = idx & 7;

  const char* ap = ws + WS_ACTF + ((size_t)(14 * w) * 128 + 4 * B) * 1024 + lane * 16;
  const char* wp = ws + WS_WDF + ((size_t)(14 * w) * 32 + 4 * dtile) * 1024 + lane * 16;

  const int rowp = t >> 2, dseg = (t & 3) * 16;
  const int rho = rowp >> 4, cx = rowp & 15;
  const int dglob = 64 * dtile + dseg;
  u32x4 x1a, x1b;
  {
    const int kb = dglob >> 5, g = (dglob & 31) >> 3;
    const char* xp = ws + WS_X1F + ((size_t)(kb * 128 + 4 * B + rho) * 64 + g * 16 + cx) * 16;
    x1a = *(const u32x4*)xp;
    x1b = *(const u32x4*)(xp + 256);
  }
  f32x4 gb[4];
  {
    const float* p = (const float*)(ws + WS_MODP) + 5 * 512 + dglob;
#pragma unroll
    for (int i = 0; i < 4; ++i) {
      f32x4 s = (*(const f32x4*)(p + 4 * i) + *(const f32x4*)(p + 3072 + 4 * i)) +
                (*(const f32x4*)(p + 6144 + 4 * i) + *(const f32x4*)(p + 9216 + 4 * i));
      gb[i] = (f32x4){rbf(s[0]), rbf(s[1]), rbf(s[2]), rbf(s[3])};
    }
  }

  f32x4 acc[4][4];
#pragma unroll
  for (int m = 0; m < 4; ++m)
#pragma unroll
    for (int n = 0; n < 4; ++n) acc[m][n] = (f32x4){0.f, 0.f, 0.f, 0.f};

  constexpr int DEPTH = 7;
  u32x4 af[DEPTH][4], wfr[DEPTH][4];
#pragma unroll
  for (int i = 0; i < DEPTH; ++i) {
#pragma unroll
    for (int n = 0; n < 4; ++n) {
      wfr[i][n] = *(const u32x4*)(wp + (size_t)i * 32 * 1024 + n * 1024);
      af[i][n] = *(const u32x4*)(ap + (size_t)i * 128 * 1024 + n * 1024);
    }
  }
#pragma unroll
  for (int i = 0; i < 14; ++i) {
    const int sl = i % DEPTH;
#pragma unroll
    for (int m = 0; m < 4; ++m)
#pragma unroll
      for (int n = 0; n < 4; ++n) acc[m][n] = MFMA16(wfr[sl][m], af[sl][n], acc[m][n]);
    if (i + DEPTH < 14) {
#pragma unroll
      for (int n = 0; n < 4; ++n) {
        wfr[sl][n] = *(const u32x4*)(wp + (size_t)(i + DEPTH) * 32 * 1024 + n * 1024);
        af[sl][n] = *(const u32x4*)(ap + (size_t)(i + DEPTH) * 128 * 1024 + n * 1024);
      }
    }
  }
  {
    const int c = lane & 15, gp = lane >> 4;
    char* yb = smem + w * G2_WAVE;
#pragma unroll
    for (int n = 0; n < 4; ++n)
#pragma unroll
      for (int m = 0; m < 4; ++m)
        *(f32x4*)(yb + (n * 16 + c) * G2_ROW + (16 * m + 4 * gp) * 4) = acc[m][n];
  }
  __syncthreads();
  {
    unsigned o[8];
    unsigned xw[8] = {x1a[0], x1a[1], x1a[2], x1a[3], x1b[0], x1b[1], x1b[2], x1b[3]};
#pragma unroll
    for (int i = 0; i < 4; ++i) {
      f32x4 y = {0.f, 0.f, 0.f, 0.f};
#pragma unroll
      for (int ww = 0; ww < 4; ++ww) y += *(const f32x4*)(smem + ww * G2_WAVE + rowp * G2_ROW + (dseg + 4 * i) * 4);
      float r0 = blo(xw[2 * i]) + rbf(gb[i][0] * rbf(y[0]));
      float r1 = bhi(xw[2 * i]) + rbf(gb[i][1] * rbf(y[1]));
      float r2 = blo(xw[2 * i + 1]) + rbf(gb[i][2] * rbf(y[2]));
      float r3 = bhi(xw[2 * i + 1]) + rbf(gb[i][3] * rbf(y[3]));
      o[2 * i] = pack2(r0, r1);
      o[2 * i + 1] = pack2(r2, r3);
    }
    const int s = 64 * B + 4 * cx + rho;
    u16* dst = out + (size_t)s * D_DIM + dglob;
    *(u32x4*)dst = (u32x4){o[0], o[1], o[2], o[3]};
    *(u32x4*)(dst + 8) = (u32x4){o[4], o[5], o[6], o[7]};
  }
}

extern "C" void kernel_launch(void* const* d_in, const int* in_sizes, int n_in, void* d_out, int out_size,
                              void* d_ws, size_t ws_size, hipStream_t stream) {
  const u16* q = (const u16*)d_in[0];
  const u16* k = (const u16*)d_in[1];
  const u16* v = (const u16*)d_in[2];
  const u16* shift_k = (const u16*)d_in[3];
  const u16* ssm_k = (const u16*)d_in[4];
  const u16* ssm_D = (const u16*)d_in[5];
  const u16* Wg = (const u16*)d_in[7];
  const u16* Wu = (const u16*)d_in[8];
  const u16* Wd = (const u16*)d_in[9];
  const u16* cvec = (const u16*)d_in[10];
  const u16* w_mod = (const u16*)d_in[11];
  char* ws = (char*)d_ws;
  (void)hipFuncSetAttribute((const void*)k1_conv, hipFuncAttributeMaxDynamicSharedMemorySize, K1_LDS);
  (void)hipFuncSetAttribute((const void*)k2_gemm1, hipFuncAttributeMaxDynamicSharedMemorySize, G1_LDS);
  (void)hipFuncSetAttribute((const void*)k3_gemm2, hipFuncAttributeMaxDynamicSharedMemorySize, G2_LDS);
  k0_prep<<<256, 256, 0, stream>>>(q, k, v, shift_k, cvec, w_mod, ws);
  k1_conv<<<256, 512, K1_LDS, stream>>>(q, ssm_k, ssm_D, Wg, Wu, Wd, ws);
  k2_gemm1<<<256, 256, G1_LDS, stream>>>(ws);
  k3_gemm2<<<256, 256, G2_LDS, stream>>>(ws, (u16*)d_out);
}
```

```cpp
#include <hip/hip_runtime.h>
#include <hip/hip_bf16.h>
#include <stdint.h>

typedef __attribute__((ext_vector_type(2))) float f32x2;
typedef __attribute__((ext_vector_type(4))) float f32x4;
typedef __attribute__((ext_vector_type(2))) __bf16 bf16x2;
typedef __attribute__((ext_vector_type(8))) __bf16 bf16x8;
typedef __attribute__((ext_vector_type(4))) unsigned u32x4;
typedef __attribute__((ext_vector_type(2))) unsigned u32x2;
typedef unsigned short u16;

#define S_LEN 2048
#define D_DIM 512
#define F_DIM 1792

#define WS_KVT   0u
#define WS_STAT1 (WS_KVT + 2097152u)
#define WS_MODP  (WS_STAT1 + 16384u)
#define WS_X1T   (WS_MODP + 49152u + 114688u)
#define WS_W1F   (WS_X1T + 2097152u)
#define WS_WDF   (WS_W1F + 3670016u)
#define WS_ACTF  (WS_WDF + 1835008u)
#define WS_XT    (WS_ACTF + 7340032u)
#define WS_GB    (WS_XT + 2097152u)
#define WS_CSB   (WS_GB + 2048u)
#define WS_CNT   (WS_CSB + 458752u)

__device__ __forceinline__ unsigned pack2(float a, float b) {
  f32x2 v = {a, b};
  bf16x2 r = __builtin_convertvector(v, bf16x2);
  return __builtin_bit_cast(unsigned, r);
}
__device__ __forceinline__ float rbf(float a) { return (float)(__bf16)a; }
__device__ __forceinline__ float blo(unsigned w) { return __builtin_bit_cast(float, w << 16); }
__device__ __forceinline__ float bhi(unsigned w) { return __builtin_bit_cast(float, w & 0xffff0000u); }
__device__ __forceinline__ float bf2f(u16 h) { return __builtin_bit_cast(float, (unsigned)h << 16); }
__device__ __forceinline__ f32x2 unpk(unsigned w) { return (f32x2){blo(w), bhi(w)}; }
__device__ __forceinline__ f32x2 rbf2(f32x2 v) { return unpk(pack2(v[0], v[1])); }

template <int CTRL>
__device__ __forceinline__ float dpp_add(float v) {
  return v + __builtin_bit_cast(float, __builtin_amdgcn_update_dpp(0, __builtin_bit_cast(int, v), CTRL, 0xf, 0xf, true));
}
__device__ __forceinline__ float xor16_sum(float v) {
  unsigned u = __builtin_bit_cast(unsigned, v);
  auto r = __builtin_amdgcn_permlane16_swap(u, u, false, false);
  return __builtin_bit_cast(float, (unsigned)r[0]) + __builtin_bit_cast(float, (unsigned)r[1]);
}
__device__ __forceinline__ float xor32_sum(float v) {
  unsigned u = __builtin_bit_cast(unsigned, v);
  auto r = __builtin_amdgcn_permlane32_swap(u, u, false, false);
  return __builtin_bit_cast(float, (unsigned)r[0]) + __builtin_bit_cast(float, (unsigned)r[1]);
}
typedef __attribute__((ext_vector_type(4))) short s16x4;
__device__ __forceinline__ u32x2 lds_tr(const char* p) {
  s16x4 r = __builtin_amdgcn_ds_read_tr16_b64_v4i16((__attribute__((address_space(3))) s16x4*)p);
  return __builtin_bit_cast(u32x2, r);
}
__device__ __forceinline__ float wave_sum(float v) {
  v = dpp_add<0x111>(v);
  v = dpp_add<0x112>(v);
  v = dpp_add<0x114>(v);
  v = dpp_add<0x118>(v);
  float r0 = __builtin_bit_cast(float, __builtin_amdgcn_readlane(__builtin_bit_cast(int, v), 15));
  float r1 = __builtin_bit_cast(float, __builtin_amdgcn_readlane(__builtin_bit_cast(int, v), 31));
  float r2 = __builtin_bit_cast(float, __builtin_amdgcn_readlane(__builtin_bit_cast(int, v), 47));
  float r3 = __builtin_bit_cast(float, __builtin_amdgcn_readlane(__builtin_bit_cast(int, v), 63));
  return (r0 + r1) + (r2 + r3);
}

#define MFMA16(a, b, c) __builtin_amdgcn_mfma_f32_16x16x32_bf16(__builtin_bit_cast(bf16x8, a), __builtin_bit_cast(bf16x8, b), c, 0, 0, 0)
__device__ __forceinline__ void st16_wt(void* p, u32x4 v) {
  asm volatile("global_store_dwordx4 %0, %1, off sc1\n\ts_nop 1" ::"v"(p), "v"(v) : "memory");
}
#define K1_CODE_BYTES 12864
#define K2_CODE_BYTES 15296
#define SCHED_FENCE() __builtin_amdgcn_sched_barrier(0)
#define MEM_FENCE() asm volatile("" ::: "memory")
#define LGKM0_BAR() do { SCHED_FENCE(); asm volatile("s_waitcnt lgkmcnt(0)" ::: "memory"); __builtin_amdgcn_s_barrier(); MEM_FENCE(); SCHED_FENCE(); } while (0)

__device__ __forceinline__ void conv_sync(unsigned* cnt, unsigned target, int lane) {
  SCHED_FENCE();
  asm volatile("s_waitcnt lgkmcnt(0)" ::: "memory");
  if (lane == 0) __hip_atomic_fetch_add(cnt, 1u, __ATOMIC_RELAXED, __HIP_MEMORY_SCOPE_WORKGROUP);
  while (__hip_atomic_load(cnt, __ATOMIC_RELAXED, __HIP_MEMORY_SCOPE_WORKGROUP) < target) __builtin_amdgcn_s_sleep(1);
  asm volatile("" ::: "memory");
  SCHED_FENCE();
}

#define K0_TS 34
extern "C" __global__ __launch_bounds__(896) void k0_prep(const u16* __restrict__ q, const u16* __restrict__ k,
                                                const u16* __restrict__ v, const u16* __restrict__ shift_k,
                                                const u16* __restrict__ cvec, const u16* __restrict__ w_mod,
                                                char* __restrict__ ws) {
  __shared__ __attribute__((aligned(16))) unsigned sm[2 * 64 * K0_TS + 4];
  const int t = threadIdx.x, b = blockIdx.x;
  const int lane = t & 63;
  const int wv = __builtin_amdgcn_readfirstlane(t >> 6);
  const int dt = b & 7, st = b >> 3;

  if (wv < 8) {
    const int cq = t & 15, rg = t >> 4;
    const int r0 = 64 * st + 2 * rg;
    const int dbase = 64 * dt + 4 * cq;
    u32x2 kr[5], vr[2], qr[2];
    u32x4 wk[2];
#pragma unroll
    for (int i = 0; i < 5; ++i) {
      int r = r0 - 3 + i;
      r = r < 0 ? 0 : r;
      kr[i] = *(const u32x2*)(k + (size_t)r * D_DIM + dbase);
    }
#pragma unroll
    for (int i = 0; i < 2; ++i) vr[i] = *(const u32x2*)(v + (size_t)(r0 + i) * D_DIM + dbase);
#pragma unroll
    for (int i = 0; i < 2; ++i) wk[i] = *(const u32x4*)(shift_k + (size_t)dbase * 4 + i * 8);
#pragma unroll
    for (int i = 0; i < 2; ++i) qr[i] = *(const u32x2*)(q + (size_t)(r0 + i) * D_DIM + dbase);
    if (t == 0) sm[2 * 64 * K0_TS] = 0u;
    LGKM0_BAR();
#pragma unroll
    for (int i = 0; i < 3; ++i)
      if (r0 - 3 + i < 0) kr[i] = (u32x2){0u, 0u};
#pragma unroll
    for (int p = 0; p < 2; ++p) {
      f32x2 w0 = {blo(wk[p][0]), blo(wk[p][2])}, w1 = {bhi(wk[p][0]), bhi(wk[p][2])};
      f32x2 w2 = {blo(wk[p][1]), blo(wk[p][3])}, w3 = {bhi(wk[p][1]), bhi(wk[p][3])};
      f32x2 kk[5];
#pragma unroll
      for (int r = 0; r < 5; ++r) kk[r] = unpk(kr[r][p]);
      f32x2 res[2];
#pragma unroll
      for (int r = 0; r < 2; ++r) {
        f32x2 a = __builtin_elementwise_fma(kk[r + 1], w2, kk[r] * w3);
        a = __builtin_elementwise_fma(kk[r + 2], w1, a);
        a = __builtin_elementwise_fma(kk[r + 3], w0, a);
        res[r] = a * unpk(vr[r][p]);
      }
      sm[(4 * cq + 2 * p) * K0_TS + (31 - rg)] = pack2(res[1][0], res[0][0]);
      sm[(4 * cq + 2 * p + 1) * K0_TS + (31 - rg)] = pack2(res[1][1], res[0][1]);
      sm[(64 + 4 * cq + 2 * p) * K0_TS + rg] = __builtin_amdgcn_perm(qr[1][p], qr[0][p], 0x05040100u);
      sm[(64 + 4 * cq + 2 * p + 1) * K0_TS + rg] = __builtin_amdgcn_perm(qr[1][p], qr[0][p], 0x07060302u);
    }
    conv_sync(sm + 2 * 64 * K0_TS, 8u, lane);
    {
      const int c = t >> 3, seg = t & 7;
      const u32x2* p0 = (const u32x2*)(sm + c * K0_TS + seg * 4);
      const u32x2* p1 = (const u32x2*)(sm + (64 + c) * K0_TS + seg * 4);
      u32x2 a0 = p0[0], a1 = p0[1], b0 = p1[0], b1 = p1[1];
      st16_wt(ws + WS_KVT + ((size_t)(64 * dt + c) * S_LEN + (S_LEN - 64 * (st + 1)) + seg * 8) * 2, (u32x4){a0[0], a0[1], a1[0], a1[1]});
      st16_wt(ws + WS_XT + ((size_t)(64 * dt + c) * S_LEN + 64 * st + seg * 8) * 2, (u32x4){b0[0], b0[1], b1[0], b1[1]});
    }
  } else if (wv < 12) {
    const int row = 8 * b + 2 * (wv - 8);
    u32x4 qr[2];
#pragma unroll
    for (int i = 0; i < 2; ++i) qr[i] = *(const u32x4*)(q + (size_t)(row + i) * D_DIM + lane * 8);
    SCHED_FENCE();
    __builtin_amdgcn_s_barrier();
    SCHED_FENCE();
    u32x2 o2;
#pragma unroll
    for (int i = 0; i < 2; ++i) {
      float x[8];
#pragma unroll
      for (int j = 0; j < 4; ++j) { x[2 * j] = blo(qr[i][j]); x[2 * j + 1] = bhi(qr[i][j]); }
      float s = ((x[0] + x[1]) + (x[2] + x[3])) + ((x[4] + x[5]) + (x[6] + x[7]));
      float mean = wave_sum(s) * (1.0f / D_DIM);
      float ss = 0.f;
#pragma unroll
      for (int j = 0; j < 8; ++j) { float d = x[j] - mean; ss += d * d; }
      float var = rbf(wave_sum(ss) * (1.0f / D_DIM));
      o2[i] = pack2(mean, __builtin_amdgcn_sqrtf(rbf(var + 1e-5f)));
    }
    if (lane == 0) *(u32x2*)(ws + WS_STAT1 + (size_t)row * 4) = o2;
  } else if (b < 192) {
    const int strip = b % 48, part = 2 * (b / 48) + (wv - 12);
    const int o = lane & 7, rr = lane >> 3;
    const u16 cv = cvec[64 * part + lane];
    u32x4 wm[8];
#pragma unroll
    for (int i = 0; i < 8; ++i)
      wm[i] = *(const u32x4*)(w_mod + (size_t)(64 * part + rr + 8 * i) * 3072 + 64 * strip + 8 * o);
    SCHED_FENCE();
    __builtin_amdgcn_s_barrier();
    SCHED_FENCE();
    const float c = bf2f(cv);
    const int scl = __builtin_bit_cast(int, rbf(c * __builtin_amdgcn_rcpf(1.0f + __expf(-c))));
    float sc[8];
#pragma unroll
    for (int i = 0; i < 8; ++i) sc[i] = __builtin_bit_cast(float, __builtin_amdgcn_ds_bpermute((rr + 8 * i) * 4, scl));
    float acc[8];
#pragma unroll
    for (int j = 0; j < 8; ++j) acc[j] = 0.f;
#pragma unroll
    for (int i = 0; i < 8; ++i) {
#pragma unroll
      for (int j = 0; j < 4; ++j) {
        acc[2 * j] += sc[i] * blo(wm[i][j]);
        acc[2 * j + 1] += sc[i] * bhi(wm[i][j]);
      }
    }
#pragma unroll
    for (int j = 0; j < 8; ++j) acc[j] = xor32_sum(xor16_sum(dpp_add<0x118>(acc[j])));
    if ((lane & 8) && lane < 32) {
      const int hh = lane >> 4;
      f32x4 o4 = hh ? (f32x4){acc[4], acc[5], acc[6], acc[7]} : (f32x4){acc[0], acc[1], acc[2], acc[3]};
      *(f32x4*)(ws + WS_MODP + ((size_t)part * 3072 + 64 * strip + 8 * (lane & 7) + 4 * hh) * 4) = o4;
    }
  } else {
    __builtin_amdgcn_s_barrier();
    unsigned long long a1, a2;
    int o1, o2;
    asm volatile("s_getpc_b64 %0\n\ts_mov_b32 %1, k1_conv@rel32@lo+4" : "=s"(a1), "=s"(o1));
    asm volatile("s_getpc_b64 %0\n\ts_mov_b32 %1, k2_mlp@rel32@lo+4" : "=s"(a2), "=s"(o2));
    a1 += (long long)o1; a2 += (long long)o2;
    const int off = (((b >> 3) & 1) * 128 + (t - 768)) * 64;
    const unsigned long long p1 = a1 + (off < K1_CODE_BYTES ? off : 0);
    const unsigned long long p2 = a2 + (off < K2_CODE_BYTES ? off : 0);
    unsigned r1, r2;
    asm volatile("global_load_dword %0, %2, off\n\tglobal_load_dword %1, %3, off\n\ts_waitcnt vmcnt(0)"
                 : "=&v"(r1), "=&v"(r2) : "v"(p1), "v"(p2) : "memory");
    if (b == 255 && lane < 16 && wv == 12)
      __hip_atomic_store((unsigned*)(ws + WS_CNT + lane * 256), 0u, __ATOMIC_RELAXED, __HIP_MEMORY_SCOPE_AGENT);
  }
}

__device__ __forceinline__ float mod_val(const float* __restrict__ p, int idx) {
  float m[8];
#pragma unroll
  for (int i = 0; i < 8; ++i) m[i] = p[i * 3072 + idx];
  return rbf(((m[0] + m[1]) + (m[2] + m[3])) + ((m[4] + m[5]) + (m[6] + m[7])));
}

#define HC_STRIDE 272
#define HC_BYTES (8 * HC_STRIDE * 16 + 256)
#define UT_ROWS 48
#define UT_BYTES (8 * UT_ROWS * 16 + 64)
#define VT_BYTES 2048
#define Y_ROW 272
#define Y_WAVE (32 * Y_ROW)
#define Y_CH (4 * Y_WAVE + 16)
#define K1_OFF_H 0
#define K1_OFF_U (2 * HC_BYTES)
#define K1_OFF_V (K1_OFF_U + 2 * UT_BYTES)
#define K1_OFF_Y (K1_OFF_V + 2 * VT_BYTES)
#define K1_OFF_M (K1_OFF_Y + 2 * Y_CH)
#define K1_OFF_S (K1_OFF_M + 1024 + 64)
#define K1_LDS (K1_OFF_S + 4096)

struct ConvPre {
  u32x4 x, kv, st[2];
  float sa, c1, Dd;
  float h1[8], kd[8];
  float sp;
  u16 dD;
  __device__ __forceinline__ void init() {
    const int mv = __builtin_bit_cast(int, rbf(xor32_sum(xor16_sum(dpp_add<0x118>(sp)))));
    const float ga = __builtin_bit_cast(float, __builtin_amdgcn_readlane(mv, 10));
    sa = __builtin_bit_cast(float, __builtin_amdgcn_readlane(mv, 8)) * ga;
    c1 = rbf(1.0f + __builtin_bit_cast(float, __builtin_amdgcn_readlane(mv, 9))) * ga;
    Dd = bf2f(dD);
  }
  __device__ __forceinline__ void step(int i) {
    const float xv = (i & 1) ? bhi(x[i >> 1]) : blo(x[i >> 1]);
    const float kvv = ((7 - i) & 1) ? bhi(kv[(7 - i) >> 1]) : blo(kv[(7 - i) >> 1]);
    const unsigned sw = st[i >> 2][i & 3];
    const float n1 = (xv - blo(sw)) * __builtin_amdgcn_rcpf(bhi(sw));
    h1[i] = __builtin_fmaf(n1, c1, sa);
    kd[i] = __builtin_fmaf(h1[i], kvv * Dd, xv);
  }
};
#define PIN4(a) asm volatile("" : "+v"(a[0]), "+v"(a[1]), "+v"(a[2]), "+v"(a[3]))
#define PIN2(a) asm volatile("" : "+v"(a[0]), "+v"(a[1]))
#define PINPRE(i) asm volatile("" : "+v"(pre.h1[i]), "+v"(pre.kd[i]))
#define CONV_SCHED(nm) do { _Pragma("unroll") for (int _i = 0; _i < (nm); ++_i) { \
    __builtin_amdgcn_sched_group_barrier(0x008, 1, 0); __builtin_amdgcn_sched_group_barrier(0x100, 1, 0); \
    __builtin_amdgcn_sched_group_barrier(0x002, 3, 0); } } while (0)

__device__ __forceinline__ void conv_w0(const char* hb, const char* ub, const char* vb, bool odd, f32x4 (&a0)[4], f32x4 (&a1)[4], f32x4 (&a3)[2], ConvPre& pre) {
  const u32x4 b0_0 = *(const u32x4*)(ub + (0 * UT_ROWS + 0) * 16);
  const u32x4 f2 = *(const u32x4*)(hb + 64);
  const u32x4 f3 = *(const u32x4*)(hb + 96);
  const u32x4 b1_0 = *(const u32x4*)(ub + (0 * UT_ROWS + -16) * 16);
  const u32x4 b0_1 = *(const u32x4*)(ub + (4 * UT_ROWS + 0) * 16);
  const u32x4 f4 = *(const u32x4*)(hb + 128);
  const u32x4 f5 = *(const u32x4*)(hb + 160);
  const u32x4 b1_1 = *(const u32x4*)(ub + (4 * UT_ROWS + -16) * 16);
  SCHED_FENCE();
  a0[2] = MFMA16(f2, b0_0, a0[2]);
  a0[3] = MFMA16(f3, b0_0, a0[3]);
  a1[2] = MFMA16(f2, b1_0, a1[2]);
  a1[3] = MFMA16(f3, b1_0, a1[3]);
  CONV_SCHED(4);
  PIN4(a0);
  PIN4(a1);
  SCHED_FENCE();
  a0[0] = MFMA16(f2, b0_1, a0[0]);
  const u32x4 b0_2 = *(const u32x4*)(ub + (0 * UT_ROWS + 1) * 16);
  a0[1] = MFMA16(f3, b0_1, a0[1]);
  const u32x4 f6 = *(const u32x4*)(hb + 192);
  a0[2] = MFMA16(f4, b0_1, a0[2]);
  const u32x4 f7 = *(const u32x4*)(hb + 224);
  a0[3] = MFMA16(f5, b0_1, a0[3]);
  const u32x4 b1_2 = *(const u32x4*)(ub + (0 * UT_ROWS + -15) * 16);
  a1[0] = MFMA16(f2, b1_1, a1[0]);
  a1[1] = MFMA16(f3, b1_1, a1[1]);
  a1[2] = MFMA16(f4, b1_1, a1[2]);
  a1[3] = MFMA16(f5, b1_1, a1[3]);
  CONV_SCHED(8);
  PIN4(a0);
  PIN4(a1);
  SCHED_FENCE();
  a0[0] = MFMA16(f4, b0_2, a0[0]);
  const u32x4 b0_3 = *(const u32x4*)(ub + (4 * UT_ROWS + 1) * 16);
  a0[1] = MFMA16(f5, b0_2, a0[1]);
  const u32x4 f8 = *(const u32x4*)(hb + 256);
  a0[2] = MFMA16(f6, b0_2, a0[2]);
  const u32x4 f9 = *(const u32x4*)(hb + 288);
  a0[3] = MFMA16(f7, b0_2, a0[3]);
  const u32x4 b1_3 = *(const u32x4*)(ub + (4 * UT_ROWS + -15) * 16);
  a1[0] = MFMA16(f4, b1_2, a1[0]);
  a1[1] = MFMA16(f5, b1_2, a1[1]);
  a1[2] = MFMA16(f6, b1_2, a1[2]);
  a1[3] = MFMA16(f7, b1_2, a1[3]);
  CONV_SCHED(8);
  PIN4(a0);
  PIN4(a1);
  SCHED_FENCE();
  a0[0] = MFMA16(f6, b0_3, a0[0]);
  const u32x4 b0_4 = *(const u32x4*)(ub + (0 * UT_ROWS + 2) * 16);
  a0[1] = MFMA16(f7, b0_3, a0[1]);
  const u32x4 f10 = *(const u32x4*)(hb + 320);
  a0[2] = MFMA16(f8, b0_3, a0[2]);
  const u32x4 f11 = *(const u32x4*)(hb + 352);
  a0[3] = MFMA16(f9, b0_3, a0[3]);
  const u32x4 b1_4 = *(const u32x4*)(ub + (0 * UT_ROWS + -14) * 16);
  a1[0] = MFMA16(f6, b1_3, a1[0]);
  a1[1] = MFMA16(f7, b1_3, a1[1]);
  a1[2] = MFMA16(f8, b1_3, a1[2]);
  a1[3] = MFMA16(f9, b1_3, a1[3]);
  CONV_SCHED(8);
  PIN4(a0);
  PIN4(a1);
  SCHED_FENCE();
  a0[0] = MFMA16(f8, b0_4, a0[0]);
  const u32x4 b0_5 = *(const u32x4*)(ub + (4 * UT_ROWS + 2) * 16);
  a0[1] = MFMA16(f9, b0_4, a0[1]);
  const u32x4 f12 = *(const u32x4*)(hb + 384);
  a0[2] = MFMA16(f10, b0_4, a0[2]);
  const u32x4 f13 = *(const u32x4*)(hb + 416);
  a0[3] = MFMA16(f11, b0_4, a0[3]);
  const u32x4 b1_5 = *(const u32x4*)(ub + (4 * UT_ROWS + -14) * 16);
  a1[0] = MFMA16(f8, b1_4, a1[0]);
  a1[1] = MFMA16(f9, b1_4, a1[1]);
  a1[2] = MFMA16(f10, b1_4, a1[2]);
  a1[3] = MFMA16(f11, b1_4, a1[3]);
  pre.init();
  pre.step(0);
  CONV_SCHED(8);
  PIN4(a0);
  PIN4(a1);
  PINPRE(0);
  SCHED_FENCE();
  a0[0] = MFMA16(f10, b0_5, a0[0]);
  const u32x4 b0_6 = *(const u32x4*)(ub + (0 * UT_ROWS + 3) * 16);
  a0[1] = MFMA16(f11, b0_5, a0[1]);
  const u32x4 f14 = *(const u32x4*)(hb + 448);
  a0[2] = MFMA16(f12, b0_5, a0[2]);
  const u32x4 f15 = *(const u32x4*)(hb + 480);
  a0[3] = MFMA16(f13, b0_5, a0[3]);
  const u32x4 b1_6 = *(const u32x4*)(ub + (0 * UT_ROWS + -13) * 16);
  a1[0] = MFMA16(f10, b1_5, a1[0]);
  a1[1] = MFMA16(f11, b1_5, a1[1]);
  a1[2] = MFMA16(f12, b1_5, a1[2]);
  a1[3] = MFMA16(f13, b1_5, a1[3]);
  pre.step(1);
  CONV_SCHED(8);
  PIN4(a0);
  PIN4(a1);
  PINPRE(1);
  SCHED_FENCE();
  a0[0] = MFMA16(f12, b0_6, a0[0]);
  const u32x4 b0_7 = *(const u32x4*)(ub + (4 * UT_ROWS + 3) * 16);
  a0[1] = MFMA16(f13, b0_6, a0[1]);
  const u32x4 f16 = *(const u32x4*)(hb + 512);
  a0[2] = MFMA16(f14, b0_6, a0[2]);
  const u32x4 f17 = *(const u32x4*)(hb + 544);
  a0[3] = MFMA16(f15, b0_6, a0[3]);
  const u32x4 b1_7 = *(const u32x4*)(ub + (4 * UT_ROWS + -13) * 16);
  a1[0] = MFMA16(f12, b1_6, a1[0]);
  a1[1] = MFMA16(f13, b1_6, a1[1]);
  a1[2] = MFMA16(f14, b1_6, a1[2]);
  a1[3] = MFMA16(f15, b1_6, a1[3]);
  pre.step(2);
  pre.step(3);
  CONV_SCHED(8);
  PIN4(a0);
  PIN4(a1);
  PINPRE(2);
  PINPRE(3);
  SCHED_FENCE();
  a0[0] = MFMA16(f14, b0_7, a0[0]);
  const u32x4 b0_8 = *(const u32x4*)(ub + (0 * UT_ROWS + 4) * 16);
  a0[1] = MFMA16(f15, b0_7, a0[1]);
  const u32x4 f18 = *(const u32x4*)(hb + 576);
  a0[2] = MFMA16(f16, b0_7, a0[2]);
  const u32x4 f19 = *(const u32x4*)(hb + 608);
  a0[3] = MFMA16(f17, b0_7, a0[3]);
  const u32x4 b1_8 = *(const u32x4*)(ub + (0 * UT_ROWS + -12) * 16);
  a1[0] = MFMA16(f14, b1_7, a1[0]);
  a1[1] = MFMA16(f15, b1_7, a1[1]);
  a1[2] = MFMA16(f16, b1_7, a1[2]);
  a1[3] = MFMA16(f17, b1_7, a1[3]);
  pre.step(4);
  CONV_SCHED(8);
  PIN4(a0);
  PIN4(a1);
  PINPRE(4);
  SCHED_FENCE();
  a0[0] = MFMA16(f16, b0_8, a0[0]);
  const u32x4 b0_9 = *(const u32x4*)(ub + (4 * UT_ROWS + 4) * 16);
  a0[1] = MFMA16(f17, b0_8, a0[1]);
  const u32x4 f20 = *(const u32x4*)(hb + 640);
  a0[2] = MFMA16(f18, b0_8, a0[2]);
  const u32x4 f21 = *(const u32x4*)(hb + 672);
  a0[3] = MFMA16(f19, b0_8, a0[3]);
  const u32x4 b1_9 = *(const u32x4*)(ub + (4 * UT_ROWS + -12) * 16);
  a1[0] = MFMA16(f16, b1_8, a1[0]);
  a1[1] = MFMA16(f17, b1_8, a1[1]);
  a1[2] = MFMA16(f18, b1_8, a1[2]);
  a1[3] = MFMA16(f19, b1_8, a1[3]);
  pre.step(5);
  CONV_SCHED(8);
  PIN4(a0);
  PIN4(a1);
  PINPRE(5);
  SCHED_FENCE();
  a0[0] = MFMA16(f18, b0_9, a0[0]);
  a0[1] = MFMA16(f19, b0_9, a0[1]);
  a0[2] = MFMA16(f20, b0_9, a0[2]);
  a0[3] = MFMA16(f21, b0_9, a0[3]);
  a1[0] = MFMA16(f18, b1_9, a1[0]);
  a1[1] = MFMA16(f19, b1_9, a1[1]);
  a1[2] = MFMA16(f20, b1_9, a1[2]);
  a1[3] = MFMA16(f21, b1_9, a1[3]);
  pre.step(6);
  pre.step(7);
  CONV_SCHED(8);
  PIN4(a0);
  PIN4(a1);
  PINPRE(6);
  PINPRE(7);
  SCHED_FENCE();
}
__device__ __forceinline__ void conv_w1(const char* hb, const char* ub, const char* vb, bool odd, f32x4 (&a0)[4], f32x4 (&a1)[4], f32x4 (&a3)[2], ConvPre& pre) {
  u32x4 b3_48 = *(const u32x4*)(vb + 0);
  const u32x4 f96 = *(const u32x4*)(hb + 3072);
  const u32x4 f97 = *(const u32x4*)(hb + 3104);
  u32x4 b3_49 = *(const u32x4*)(vb + 16);
  const u32x4 f98 = *(const u32x4*)(hb + 3136);
  const u32x4 f99 = *(const u32x4*)(hb + 3168);
  u32x4 b3_50 = *(const u32x4*)(vb + 32);
  const u32x4 f100 = *(const u32x4*)(hb + 3200);
  const u32x4 f101 = *(const u32x4*)(hb + 3232);
  u32x4 b3_51 = *(const u32x4*)(vb + 48);
  const u32x4 f102 = *(const u32x4*)(hb + 3264);
  const u32x4 f103 = *(const u32x4*)(hb + 3296);
  u32x4 b3_52 = *(const u32x4*)(vb + 64);
  const u32x4 f104 = *(const u32x4*)(hb + 3328);
  const u32x4 f105 = *(const u32x4*)(hb + 3360);
  SCHED_FENCE();
  if (odd) b3_48 = (u32x4){0u, 0u, 0u, 0u};
  a3[0] = MFMA16(f96, b3_48, a3[0]);
  a3[1] = MFMA16(f97, b3_48, a3[1]);
  CONV_SCHED(2);
  PIN2(a3);
  SCHED_FENCE();
  a3[0] = MFMA16(f98, b3_49, a3[0]);
  u32x4 b3_53 = *(const u32x4*)(vb + 80);
  a3[1] = MFMA16(f99, b3_49, a3[1]);
  const u32x4 f106 = *(const u32x4*)(hb + 3392);
  const u32x4 f107 = *(const u32x4*)(hb + 3424);
  CONV_SCHED(2);
  PIN2(a3);
  SCHED_FENCE();
  a3[0] = MFMA16(f100, b3_50, a3[0]);
  u32x4 b3_54 = *(const u32x4*)(vb + 96);
  a3[1] = MFMA16(f101, b3_50, a3[1]);
  const u32x4 f108 = *(const u32x4*)(hb + 3456);
  const u32x4 f109 = *(const u32x4*)(hb + 3488);
  CONV_SCHED(2);
  PIN2(a3);
  SCHED_FENCE();
  a3[0] = MFMA16(f102, b3_51, a3[0]);
  u32x4 b3_55 = *(const u32x4*)(vb + 112);
  a3[1] = MFMA16(f103, b3_51, a3[1]);
  const u32x4 f110 = *(const u32x4*)(hb + 3520);
  const u32x4 f111 = *(const u32x4*)(hb + 3552);
  CONV_SCHED(2);
  PIN2(a3);
  SCHED_FENCE();
  a3[0] = MFMA16(f104, b3_52, a3[0]);
  u32x4 b3_56 = *(const u32x4*)(vb + 128);
  a3[1] = MFMA16(f105, b3_52, a3[1]);
  const u32x4 f112 = *(const u32x4*)(hb + 3584);
  const u32x4 f113 = *(const u32x4*)(hb + 3616);
  CONV_SCHED(2);
  PIN2(a3);
  SCHED_FENCE();
  a3[0] = MFMA16(f106, b3_53, a3[0]);
  u32x4 b3_57 = *(const u32x4*)(vb + 144);
  a3[1] = MFMA16(f107, b3_53, a3[1]);
  const u32x4 f114 = *(const u32x4*)(hb + 3648);
  const u32x4 f115 = *(const u32x4*)(hb + 3680);
  CONV_SCHED(2);
  PIN2(a3);
  SCHED_FENCE();
  a3[0] = MFMA16(f108, b3_54, a3[0]);
  u32x4 b3_58 = *(const u32x4*)(vb + 160);
  a3[1] = MFMA16(f109, b3_54, a3[1]);
  const u32x4 f116 = *(const u32x4*)(hb + 3712);
  const u32x4 f117 = *(const u32x4*)(hb + 3744);
  CONV_SCHED(2);
  PIN2(a3);
  SCHED_FENCE();
  a3[0] = MFMA16(f110, b3_55, a3[0]);
  u32x4 b3_59 = *(const u32x4*)(vb + 176);
  a3[1] = MFMA16(f111, b3_55, a3[1]);
  const u32x4 f118 = *(const u32x4*)(hb + 3776);
  const u32x4 f119 = *(const u32x4*)(hb + 3808);
  CONV_SCHED(2);
  PIN2(a3);
  SCHED_FENCE();
  a3[0] = MFMA16(f112, b3_56, a3[0]);
  u32x4 b3_60 = *(const u32x4*)(vb + 192);
  a3[1] = MFMA16(f113, b3_56, a3[1]);
  const u32x4 f120 = *(const u32x4*)(hb + 3840);
  const u32x4 f121 = *(const u32x4*)(hb + 3872);
  CONV_SCHED(2);
  PIN2(a3);
  SCHED_FENCE();
  a3[0] = MFMA16(f114, b3_57, a3[0]);
  u32x4 b3_61 = *(const u32x4*)(vb + 208);
  a3[1] = MFMA16(f115, b3_57, a3[1]);
  const u32x4 f122 = *(const u32x4*)(hb + 3904);
  const u32x4 f123 = *(const u32x4*)(hb + 3936);
  CONV_SCHED(2);
  PIN2(a3);
  SCHED_FENCE();
  a3[0] = MFMA16(f116, b3_58, a3[0]);
  u32x4 b3_62 = *(const u32x4*)(vb + 224);
  a3[1] = MFMA16(f117, b3_58, a3[1]);
  const u32x4 f124 = *(const u32x4*)(hb + 3968);
  const u32x4 f125 = *(const u32x4*)(hb + 4000);
  CONV_SCHED(2);
  PIN2(a3);
  SCHED_FENCE();
  a3[0] = MFMA16(f118, b3_59, a3[0]);
  u32x4 b3_63 = *(const u32x4*)(vb + 240);
  a3[1] = MFMA16(f119, b3_59, a3[1]);
  const u32x4 f126 = *(const u32x4*)(hb + 4032);
  const u32x4 f127 = *(const u32x4*)(hb + 4064);
  CONV_SCHED(2);
  PIN2(a3);
  SCHED_FENCE();
  a3[0] = MFMA16(f120, b3_60, a3[0]);
  u32x4 b3_64 = *(const u32x4*)(vb + 256);
  a3[1] = MFMA16(f121, b3_60, a3[1]);
  const u32x4 f128 = *(const u32x4*)(hb + 4096);
  const u32x4 f129 = *(const u32x4*)(hb + 4128);
  CONV_SCHED(2);
  PIN2(a3);
  SCHED_FENCE();
  a3[0] = MFMA16(f122, b3_61, a3[0]);
  const u32x4 b0_10 = *(const u32x4*)(ub + (0 * UT_ROWS + 5) * 16);
  a3[1] = MFMA16(f123, b3_61, a3[1]);
  const u32x4 f20 = *(const u32x4*)(hb + 640);
  const u32x4 f21 = *(const u32x4*)(hb + 672);
  const u32x4 f22 = *(const u32x4*)(hb + 704);
  const u32x4 f23 = *(const u32x4*)(hb + 736);
  const u32x4 b1_10 = *(const u32x4*)(ub + (0 * UT_ROWS + -11) * 16);
  CONV_SCHED(2);
  PIN2(a3);
  SCHED_FENCE();
  a3[0] = MFMA16(f124, b3_62, a3[0]);
  a3[1] = MFMA16(f125, b3_62, a3[1]);
  CONV_SCHED(2);
  PIN2(a3);
  SCHED_FENCE();
  a3[0] = MFMA16(f126, b3_63, a3[0]);
  a3[1] = MFMA16(f127, b3_63, a3[1]);
  CONV_SCHED(2);
  PIN2(a3);
  SCHED_FENCE();
  a3[0] = MFMA16(f128, b3_64, a3[0]);
  a3[1] = MFMA16(f129, b3_64, a3[1]);
  CONV_SCHED(2);
  PIN2(a3);
  SCHED_FENCE();
  a0[0] = MFMA16(f20, b0_10, a0[0]);
  const u32x4 b0_11 = *(const u32x4*)(ub + (4 * UT_ROWS + 5) * 16);
  a0[1] = MFMA16(f21, b0_10, a0[1]);
  const u32x4 f24 = *(const u32x4*)(hb + 768);
  a0[2] = MFMA16(f22, b0_10, a0[2]);
  const u32x4 f25 = *(const u32x4*)(hb + 800);
  a0[3] = MFMA16(f23, b0_10, a0[3]);
  const u32x4 b1_11 = *(const u32x4*)(ub + (4 * UT_ROWS + -11) * 16);
  a1[0] = MFMA16(f20, b1_10, a1[0]);
  a1[1] = MFMA16(f21, b1_10, a1[1]);
  a1[2] = MFMA16(f22, b1_10, a1[2]);
  a1[3] = MFMA16(f23, b1_10, a1[3]);
  pre.init();
  pre.step(0);
  CONV_SCHED(8);
  PIN4(a0);
  PIN4(a1);
  PINPRE(0);
  SCHED_FENCE();
  a0[0] = MFMA16(f22, b0_11, a0[0]);
  const u32x4 b0_12 = *(const u32x4*)(ub + (0 * UT_ROWS + 6) * 16);
  a0[1] = MFMA16(f23, b0_11, a0[1]);
  const u32x4 f26 = *(const u32x4*)(hb + 832);
  a0[2] = MFMA16(f24, b0_11, a0[2]);
  const u32x4 f27 = *(const u32x4*)(hb + 864);
  a0[3] = MFMA16(f25, b0_11, a0[3]);
  const u32x4 b1_12 = *(const u32x4*)(ub + (0 * UT_ROWS + -10) * 16);
  a1[0] = MFMA16(f22, b1_11, a1[0]);
  a1[1] = MFMA16(f23, b1_11, a1[1]);
  a1[2] = MFMA16(f24, b1_11, a1[2]);
  a1[3] = MFMA16(f25, b1_11, a1[3]);
  pre.step(1);
  CONV_SCHED(8);
  PIN4(a0);
  PIN4(a1);
  PINPRE(1);
  SCHED_FENCE();
  a0[0] = MFMA16(f24, b0_12, a0[0]);
  const u32x4 b0_13 = *(const u32x4*)(ub + (4 * UT_ROWS + 6) * 16);
  a0[1] = MFMA16(f25, b0_12, a0[1]);
  const u32x4 f28 = *(const u32x4*)(hb + 896);
  a0[2] = MFMA16(f26, b0_12, a0[2]);
  const u32x4 f29 = *(const u32x4*)(hb + 928);
  a0[3] = MFMA16(f27, b0_12, a0[3]);
  const u32x4 b1_13 = *(const u32x4*)(ub + (4 * UT_ROWS + -10) * 16);
  a1[0] = MFMA16(f24, b1_12, a1[0]);
  a1[1] = MFMA16(f25, b1_12, a1[1]);
  a1[2] = MFMA16(f26, b1_12, a1[2]);
  a1[3] = MFMA16(f27, b1_12, a1[3]);
  pre.step(2);
  pre.step(3);
  CONV_SCHED(8);
  PIN4(a0);
  PIN4(a1);
  PINPRE(2);
  PINPRE(3);
  SCHED_FENCE();
  a0[0] = MFMA16(f26, b0_13, a0[0]);
  const u32x4 b0_14 = *(const u32x4*)(ub + (0 * UT_ROWS + 7) * 16);
  a0[1] = MFMA16(f27, b0_13, a0[1]);
  const u32x4 f30 = *(const u32x4*)(hb + 960);
  a0[2] = MFMA16(f28, b0_13, a0[2]);
  const u32x4 f31 = *(const u32x4*)(hb + 992);
  a0[3] = MFMA16(f29, b0_13, a0[3]);
  const u32x4 b1_14 = *(const u32x4*)(ub + (0 * UT_ROWS + -9) * 16);
  a1[0] = MFMA16(f26, b1_13, a1[0]);
  a1[1] = MFMA16(f27, b1_13, a1[1]);
  a1[2] = MFMA16(f28, b1_13, a1[2]);
  a1[3] = MFMA16(f29, b1_13, a1[3]);
  pre.step(4);
  CONV_SCHED(8);
  PIN4(a0);
  PIN4(a1);
  PINPRE(4);
  SCHED_FENCE();
  a0[0] = MFMA16(f28, b0_14, a0[0]);
  const u32x4 b0_15 = *(const u32x4*)(ub + (4 * UT_ROWS + 7) * 16);
  a0[1] = MFMA16(f29, b0_14, a0[1]);
  const u32x4 f32 = *(const u32x4*)(hb + 1024);
  a0[2] = MFMA16(f30, b0_14, a0[2]);
  const u32x4 f33 = *(const u32x4*)(hb + 1056);
  a0[3] = MFMA16(f31, b0_14, a0[3]);
  const u32x4 b1_15 = *(const u32x4*)(ub + (4 * UT_ROWS + -9) * 16);
  a1[0] = MFMA16(f28, b1_14, a1[0]);
  a1[1] = MFMA16(f29, b1_14, a1[1]);
  a1[2] = MFMA16(f30, b1_14, a1[2]);
  a1[3] = MFMA16(f31, b1_14, a1[3]);
  pre.step(5);
  CONV_SCHED(8);
  PIN4(a0);
  PIN4(a1);
  PINPRE(5);
  SCHED_FENCE();
  a0[0] = MFMA16(f30, b0_15, a0[0]);
  a0[1] = MFMA16(f31, b0_15, a0[1]);
  a0[2] = MFMA16(f32, b0_15, a0[2]);
  a0[3] = MFMA16(f33, b0_15, a0[3]);
  a1[0] = MFMA16(f30, b1_15, a1[0]);
  a1[1] = MFMA16(f31, b1_15, a1[1]);
  a1[2] = MFMA16(f32, b1_15, a1[2]);
  a1[3] = MFMA16(f33, b1_15, a1[3]);
  pre.step(6);
  pre.step(7);
  CONV_SCHED(8);
  PIN4(a0);
  PIN4(a1);
  PINPRE(6);
  PINPRE(7);
  SCHED_FENCE();
}
__device__ __forceinline__ void conv_w2(const char* hb, const char* ub, const char* vb, bool odd, f32x4 (&a0)[4], f32x4 (&a1)[4], f32x4 (&a3)[2], ConvPre& pre) {
  const u32x4 b1_16 = *(const u32x4*)(ub + (0 * UT_ROWS + -8) * 16);
  const u32x4 f32 = *(const u32x4*)(hb + 1024);
  const u32x4 f33 = *(const u32x4*)(hb + 1056);
  const u32x4 f34 = *(const u32x4*)(hb + 1088);
  const u32x4 f35 = *(const u32x4*)(hb + 1120);
  u32x4 b3_16 = *(const u32x4*)(vb + 0);
  const u32x4 b1_17 = *(const u32x4*)(ub + (4 * UT_ROWS + -8) * 16);
  const u32x4 f36 = *(const u32x4*)(hb + 1152);
  const u32x4 f37 = *(const u32x4*)(hb + 1184);
  u32x4 b3_17 = *(const u32x4*)(vb + 16);
  SCHED_FENCE();
  if (odd) b3_16 = (u32x4){0u, 0u, 0u, 0u};
  a1[0] = MFMA16(f32, b1_16, a1[0]);
  a1[1] = MFMA16(f33, b1_16, a1[1]);
  a1[2] = MFMA16(f34, b1_16, a1[2]);
  a1[3] = MFMA16(f35, b1_16, a1[3]);
  a3[0] = MFMA16(f32, b3_16, a3[0]);
  a3[1] = MFMA16(f33, b3_16, a3[1]);
  CONV_SCHED(6);
  PIN4(a1);
  PIN2(a3);
  SCHED_FENCE();
  a1[0] = MFMA16(f34, b1_17, a1[0]);
  const u32x4 b1_18 = *(const u32x4*)(ub + (0 * UT_ROWS + -7) * 16);
  a1[1] = MFMA16(f35, b1_17, a1[1]);
  const u32x4 f38 = *(const u32x4*)(hb + 1216);
  a1[2] = MFMA16(f36, b1_17, a1[2]);
  const u32x4 f39 = *(const u32x4*)(hb + 1248);
  a1[3] = MFMA16(f37, b1_17, a1[3]);
  u32x4 b3_18 = *(const u32x4*)(vb + 32);
  a3[0] = MFMA16(f34, b3_17, a3[0]);
  a3[1] = MFMA16(f35, b3_17, a3[1]);
  CONV_SCHED(6);
  PIN4(a1);
  PIN2(a3);
  SCHED_FENCE();
  a1[0] = MFMA16(f36, b1_18, a1[0]);
  const u32x4 b1_19 = *(const u32x4*)(ub + (4 * UT_ROWS + -7) * 16);
  a1[1] = MFMA16(f37, b1_18, a1[1]);
  const u32x4 f40 = *(const u32x4*)(hb + 1280);
  a1[2] = MFMA16(f38, b1_18, a1[2]);
  const u32x4 f41 = *(const u32x4*)(hb + 1312);
  a1[3] = MFMA16(f39, b1_18, a1[3]);
  u32x4 b3_19 = *(const u32x4*)(vb + 48);
  a3[0] = MFMA16(f36, b3_18, a3[0]);
  a3[1] = MFMA16(f37, b3_18, a3[1]);
  CONV_SCHED(6);
  PIN4(a1);
  PIN2(a3);
  SCHED_FENCE();
  a1[0] = MFMA16(f38, b1_19, a1[0]);
  const u32x4 b1_20 = *(const u32x4*)(ub + (0 * UT_ROWS + -6) * 16);
  a1[1] = MFMA16(f39, b1_19, a1[1]);
  const u32x4 f42 = *(const u32x4*)(hb + 1344);
  a1[2] = MFMA16(f40, b1_19, a1[2]);
  const u32x4 f43 = *(const u32x4*)(hb + 1376);
  a1[3] = MFMA16(f41, b1_19, a1[3]);
  u32x4 b3_20 = *(const u32x4*)(vb + 64);
  a3[0] = MFMA16(f38, b3_19, a3[0]);
  a3[1] = MFMA16(f39, b3_19, a3[1]);
  CONV_SCHED(6);
  PIN4(a1);
  PIN2(a3);
  SCHED_FENCE();
  a1[0] = MFMA16(f40, b1_20, a1[0]);
  const u32x4 b1_21 = *(const u32x4*)(ub + (4 * UT_ROWS + -6) * 16);
  a1[1] = MFMA16(f41, b1_20, a1[1]);
  const u32x4 f44 = *(const u32x4*)(hb + 1408);
  a1[2] = MFMA16(f42, b1_20, a1[2]);
  const u32x4 f45 = *(const u32x4*)(hb + 1440);
  a1[3] = MFMA16(f43, b1_20, a1[3]);
  u32x4 b3_21 = *(const u32x4*)(vb + 80);
  a3[0] = MFMA16(f40, b3_20, a3[0]);
  a3[1] = MFMA16(f41, b3_20, a3[1]);
  CONV_SCHED(6);
  PIN4(a1);
  PIN2(a3);
  SCHED_FENCE();
  a1[0] = MFMA16(f42, b1_21, a1[0]);
  const u32x4 b1_22 = *(const u32x4*)(ub + (0 * UT_ROWS + -5) * 16);
  a1[1] = MFMA16(f43, b1_21, a1[1]);
  const u32x4 f46 = *(const u32x4*)(hb + 1472);
  a1[2] = MFMA16(f44, b1_21, a1[2]);
  const u32x4 f47 = *(const u32x4*)(hb + 1504);
  a1[3] = MFMA16(f45, b1_21, a1[3]);
  u32x4 b3_22 = *(const u32x4*)(vb + 96);
  a3[0] = MFMA16(f42, b3_21, a3[0]);
  a3[1] = MFMA16(f43, b3_21, a3[1]);
  pre.init();
  pre.step(0);
  CONV_SCHED(6);
  PIN4(a1);
  PIN2(a3);
  PINPRE(0);
  SCHED_FENCE();
  a1[0] = MFMA16(f44, b1_22, a1[0]);
  const u32x4 b1_23 = *(const u32x4*)(ub + (4 * UT_ROWS + -5) * 16);
  a1[1] = MFMA16(f45, b1_22, a1[1]);
  const u32x4 f48 = *(const u32x4*)(hb + 1536);
  a1[2] = MFMA16(f46, b1_22, a1[2]);
  const u32x4 f49 = *(const u32x4*)(hb + 1568);
  a1[3] = MFMA16(f47, b1_22, a1[3]);
  u32x4 b3_23 = *(const u32x4*)(vb + 112);
  a3[0] = MFMA16(f44, b3_22, a3[0]);
  a3[1] = MFMA16(f45, b3_22, a3[1]);
  pre.step(1);
  CONV_SCHED(6);
  PIN4(a1);
  PIN2(a3);
  PINPRE(1);
  SCHED_FENCE();
  a1[0] = MFMA16(f46, b1_23, a1[0]);
  const u32x4 b1_24 = *(const u32x4*)(ub + (0 * UT_ROWS + -4) * 16);
  a1[1] = MFMA16(f47, b1_23, a1[1]);
  const u32x4 f50 = *(const u32x4*)(hb + 1600);
  a1[2] = MFMA16(f48, b1_23, a1[2]);
  const u32x4 f51 = *(const u32x4*)(hb + 1632);
  a1[3] = MFMA16(f49, b1_23, a1[3]);
  u32x4 b3_24 = *(const u32x4*)(vb + 128);
  a3[0] = MFMA16(f46, b3_23, a3[0]);
  a3[1] = MFMA16(f47, b3_23, a3[1]);
  pre.step(2);
  CONV_SCHED(6);
  PIN4(a1);
  PIN2(a3);
  PINPRE(2);
  SCHED_FENCE();
  a1[0] = MFMA16(f48, b1_24, a1[0]);
  const u32x4 b1_25 = *(const u32x4*)(ub + (4 * UT_ROWS + -4) * 16);
  a1[1] = MFMA16(f49, b1_24, a1[1]);
  const u32x4 f52 = *(const u32x4*)(hb + 1664);
  a1[2] = MFMA16(f50, b1_24, a1[2]);
  const u32x4 f53 = *(const u32x4*)(hb + 1696);
  a1[3] = MFMA16(f51, b1_24, a1[3]);
  u32x4 b3_25 = *(const u32x4*)(vb + 144);
  a3[0] = MFMA16(f48, b3_24, a3[0]);
  a3[1] = MFMA16(f49, b3_24, a3[1]);
  pre.step(3);
  CONV_SCHED(6);
  PIN4(a1);
  PIN2(a3);
  PINPRE(3);
  SCHED_FENCE();
  a1[0] = MFMA16(f50, b1_25, a1[0]);
  const u32x4 b1_26 = *(const u32x4*)(ub + (0 * UT_ROWS + -3) * 16);
  a1[1] = MFMA16(f51, b1_25, a1[1]);
  const u32x4 f54 = *(const u32x4*)(hb + 1728);
  a1[2] = MFMA16(f52, b1_25, a1[2]);
  const u32x4 f55 = *(const u32x4*)(hb + 1760);
  a1[3] = MFMA16(f53, b1_25, a1[3]);
  u32x4 b3_26 = *(const u32x4*)(vb + 160);
  a3[0] = MFMA16(f50, b3_25, a3[0]);
  a3[1] = MFMA16(f51, b3_25, a3[1]);
  pre.step(4);
  CONV_SCHED(6);
  PIN4(a1);
  PIN2(a3);
  PINPRE(4);
  SCHED_FENCE();
  a1[0] = MFMA16(f52, b1_26, a1[0]);
  const u32x4 b1_27 = *(const u32x4*)(ub + (4 * UT_ROWS + -3) * 16);
  a1[1] = MFMA16(f53, b1_26, a1[1]);
  const u32x4 f56 = *(const u32x4*)(hb + 1792);
  a1[2] = MFMA16(f54, b1_26, a1[2]);
  const u32x4 f57 = *(const u32x4*)(hb + 1824);
  a1[3] = MFMA16(f55, b1_26, a1[3]);
  u32x4 b3_27 = *(const u32x4*)(vb + 176);
  a3[0] = MFMA16(f52, b3_26, a3[0]);
  a3[1] = MFMA16(f53, b3_26, a3[1]);
  pre.step(5);
  CONV_SCHED(6);
  PIN4(a1);
  PIN2(a3);
  PINPRE(5);
  SCHED_FENCE();
  a1[0] = MFMA16(f54, b1_27, a1[0]);
  const u32x4 b1_28 = *(const u32x4*)(ub + (0 * UT_ROWS + -2) * 16);
  a1[1] = MFMA16(f55, b1_27, a1[1]);
  const u32x4 f58 = *(const u32x4*)(hb + 1856);
  a1[2] = MFMA16(f56, b1_27, a1[2]);
  const u32x4 f59 = *(const u32x4*)(hb + 1888);
  a1[3] = MFMA16(f57, b1_27, a1[3]);
  u32x4 b3_28 = *(const u32x4*)(vb + 192);
  a3[0] = MFMA16(f54, b3_27, a3[0]);
  a3[1] = MFMA16(f55, b3_27, a3[1]);
  pre.step(6);
  CONV_SCHED(6);
  PIN4(a1);
  PIN2(a3);
  PINPRE(6);
  SCHED_FENCE();
  a1[0] = MFMA16(f56, b1_28, a1[0]);
  a1[1] = MFMA16(f57, b1_28, a1[1]);
  a1[2] = MFMA16(f58, b1_28, a1[2]);
  a1[3] = MFMA16(f59, b1_28, a1[3]);
  a3[0] = MFMA16(f56, b3_28, a3[0]);
  a3[1] = MFMA16(f57, b3_28, a3[1]);
  pre.step(7);
  CONV_SCHED(6);
  PIN4(a1);
  PIN2(a3);
  PINPRE(7);
  SCHED_FENCE();
}
__device__ __forceinline__ void conv_w3(const char* hb, const char* ub, const char* vb, bool odd, f32x4 (&a0)[4], f32x4 (&a1)[4], f32x4 (&a3)[2], ConvPre& pre) {
  const u32x4 b1_29 = *(const u32x4*)(ub + (4 * UT_ROWS + -2) * 16);
  const u32x4 f58 = *(const u32x4*)(hb + 1856);
  const u32x4 f59 = *(const u32x4*)(hb + 1888);
  const u32x4 f60 = *(const u32x4*)(hb + 1920);
  const u32x4 f61 = *(const u32x4*)(hb + 1952);
  u32x4 b3_29 = *(const u32x4*)(vb + 208);
  const u32x4 b1_30 = *(const u32x4*)(ub + (0 * UT_ROWS + -1) * 16);
  const u32x4 f62 = *(const u32x4*)(hb + 1984);
  const u32x4 f63 = *(const u32x4*)(hb + 2016);
  u32x4 b3_30 = *(const u32x4*)(vb + 224);
  SCHED_FENCE();
  a1[0] = MFMA16(f58, b1_29, a1[0]);
  a1[1] = MFMA16(f59, b1_29, a1[1]);
  a1[2] = MFMA16(f60, b1_29, a1[2]);
  a1[3] = MFMA16(f61, b1_29, a1[3]);
  a3[0] = MFMA16(f58, b3_29, a3[0]);
  a3[1] = MFMA16(f59, b3_29, a3[1]);
  CONV_SCHED(6);
  PIN4(a1);
  PIN2(a3);
  SCHED_FENCE();
  a1[0] = MFMA16(f60, b1_30, a1[0]);
  const u32x4 b1_31 = *(const u32x4*)(ub + (4 * UT_ROWS + -1) * 16);
  a1[1] = MFMA16(f61, b1_30, a1[1]);
  const u32x4 f64 = *(const u32x4*)(hb + 2048);
  a1[2] = MFMA16(f62, b1_30, a1[2]);
  const u32x4 f65 = *(const u32x4*)(hb + 2080);
  a1[3] = MFMA16(f63, b1_30, a1[3]);
  u32x4 b3_31 = *(const u32x4*)(vb + 240);
  a3[0] = MFMA16(f60, b3_30, a3[0]);
  a3[1] = MFMA16(f61, b3_30, a3[1]);
  CONV_SCHED(6);
  PIN4(a1);
  PIN2(a3);
  SCHED_FENCE();
  a1[0] = MFMA16(f62, b1_31, a1[0]);
  const u32x4 b1_32 = *(const u32x4*)(ub + (0 * UT_ROWS + 0) * 16);
  a1[1] = MFMA16(f63, b1_31, a1[1]);
  const u32x4 f66 = *(const u32x4*)(hb + 2112);
  a1[2] = MFMA16(f64, b1_31, a1[2]);
  const u32x4 f67 = *(const u32x4*)(hb + 2144);
  a1[3] = MFMA16(f65, b1_31, a1[3]);
  u32x4 b3_32 = *(const u32x4*)(vb + 256);
  a3[0] = MFMA16(f62, b3_31, a3[0]);
  a3[1] = MFMA16(f63, b3_31, a3[1]);
  CONV_SCHED(6);
  PIN4(a1);
  PIN2(a3);
  SCHED_FENCE();
  a1[0] = MFMA16(f64, b1_32, a1[0]);
  const u32x4 b1_33 = *(const u32x4*)(ub + (4 * UT_ROWS + 0) * 16);
  a1[1] = MFMA16(f65, b1_32, a1[1]);
  const u32x4 f68 = *(const u32x4*)(hb + 2176);
  a1[2] = MFMA16(f66, b1_32, a1[2]);
  const u32x4 f69 = *(const u32x4*)(hb + 2208);
  a1[3] = MFMA16(f67, b1_32, a1[3]);
  a3[0] = MFMA16(f64, b3_32, a3[0]);
  a3[1] = MFMA16(f65, b3_32, a3[1]);
  CONV_SCHED(6);
  PIN4(a1);
  PIN2(a3);
  SCHED_FENCE();
  a1[0] = MFMA16(f66, b1_33, a1[0]);
  const u32x4 b1_34 = *(const u32x4*)(ub + (0 * UT_ROWS + 1) * 16);
  a1[1] = MFMA16(f67, b1_33, a1[1]);
  const u32x4 f70 = *(const u32x4*)(hb + 2240);
  a1[2] = MFMA16(f68, b1_33, a1[2]);
  const u32x4 f71 = *(const u32x4*)(hb + 2272);
  a1[3] = MFMA16(f69, b1_33, a1[3]);
  const u32x4 b1_35 = *(const u32x4*)(ub + (4 * UT_ROWS + 1) * 16);
  const u32x4 f72 = *(const u32x4*)(hb + 2304);
  const u32x4 f73 = *(const u32x4*)(hb + 2336);
  CONV_SCHED(4);
  PIN4(a1);
  SCHED_FENCE();
  a1[0] = MFMA16(f68, b1_34, a1[0]);
  const u32x4 b1_36 = *(const u32x4*)(ub + (0 * UT_ROWS + 2) * 16);
  a1[1] = MFMA16(f69, b1_34, a1[1]);
  const u32x4 f74 = *(const u32x4*)(hb + 2368);
  a1[2] = MFMA16(f70, b1_34, a1[2]);
  const u32x4 f75 = *(const u32x4*)(hb + 2400);
  a1[3] = MFMA16(f71, b1_34, a1[3]);
  CONV_SCHED(4);
  PIN4(a1);
  SCHED_FENCE();
  a1[0] = MFMA16(f70, b1_35, a1[0]);
  const u32x4 b1_37 = *(const u32x4*)(ub + (4 * UT_ROWS + 2) * 16);
  a1[1] = MFMA16(f71, b1_35, a1[1]);
  const u32x4 f76 = *(const u32x4*)(hb + 2432);
  a1[2] = MFMA16(f72, b1_35, a1[2]);
  const u32x4 f77 = *(const u32x4*)(hb + 2464);
  a1[3] = MFMA16(f73, b1_35, a1[3]);
  CONV_SCHED(4);
  PIN4(a1);
  SCHED_FENCE();
  a1[0] = MFMA16(f72, b1_36, a1[0]);
  const u32x4 b1_38 = *(const u32x4*)(ub + (0 * UT_ROWS + 3) * 16);
  a1[1] = MFMA16(f73, b1_36, a1[1]);
  const u32x4 f78 = *(const u32x4*)(hb + 2496);
  a1[2] = MFMA16(f74, b1_36, a1[2]);
  const u32x4 f79 = *(const u32x4*)(hb + 2528);
  a1[3] = MFMA16(f75, b1_36, a1[3]);
  CONV_SCHED(4);
  PIN4(a1);
  SCHED_FENCE();
  a1[0] = MFMA16(f74, b1_37, a1[0]);
  const u32x4 b1_39 = *(const u32x4*)(ub + (4 * UT_ROWS + 3) * 16);
  a1[1] = MFMA16(f75, b1_37, a1[1]);
  const u32x4 f80 = *(const u32x4*)(hb + 2560);
  a1[2] = MFMA16(f76, b1_37, a1[2]);
  const u32x4 f81 = *(const u32x4*)(hb + 2592);
  a1[3] = MFMA16(f77, b1_37, a1[3]);
  pre.init();
  pre.step(0);
  CONV_SCHED(4);
  PIN4(a1);
  PINPRE(0);
  SCHED_FENCE();
  a1[0] = MFMA16(f76, b1_38, a1[0]);
  const u32x4 b1_40 = *(const u32x4*)(ub + (0 * UT_ROWS + 4) * 16);
  a1[1] = MFMA16(f77, b1_38, a1[1]);
  const u32x4 f82 = *(const u32x4*)(hb + 2624);
  a1[2] = MFMA16(f78, b1_38, a1[2]);
  const u32x4 f83 = *(const u32x4*)(hb + 2656);
  a1[3] = MFMA16(f79, b1_38, a1[3]);
  pre.step(1);
  CONV_SCHED(4);
  PIN4(a1);
  PINPRE(1);
  SCHED_FENCE();
  a1[0] = MFMA16(f78, b1_39, a1[0]);
  const u32x4 b1_41 = *(const u32x4*)(ub + (4 * UT_ROWS + 4) * 16);
  a1[1] = MFMA16(f79, b1_39, a1[1]);
  const u32x4 f84 = *(const u32x4*)(hb + 2688);
  a1[2] = MFMA16(f80, b1_39, a1[2]);
  const u32x4 f85 = *(const u32x4*)(hb + 2720);
  a1[3] = MFMA16(f81, b1_39, a1[3]);
  CONV_SCHED(4);
  PIN4(a1);
  SCHED_FENCE();
  a1[0] = MFMA16(f80, b1_40, a1[0]);
  const u32x4 b1_42 = *(const u32x4*)(ub + (0 * UT_ROWS + 5) * 16);
  a1[1] = MFMA16(f81, b1_40, a1[1]);
  const u32x4 f86 = *(const u32x4*)(hb + 2752);
  a1[2] = MFMA16(f82, b1_40, a1[2]);
  const u32x4 f87 = *(const u32x4*)(hb + 2784);
  a1[3] = MFMA16(f83, b1_40, a1[3]);
  pre.step(2);
  CONV_SCHED(4);
  PIN4(a1);
  PINPRE(2);
  SCHED_FENCE();
  a1[0] = MFMA16(f82, b1_41, a1[0]);
  const u32x4 b1_43 = *(const u32x4*)(ub + (4 * UT_ROWS + 5) * 16);
  a1[1] = MFMA16(f83, b1_41, a1[1]);
  const u32x4 f88 = *(const u32x4*)(hb + 2816);
  a1[2] = MFMA16(f84, b1_41, a1[2]);
  const u32x4 f89 = *(const u32x4*)(hb + 2848);
  a1[3] = MFMA16(f85, b1_41, a1[3]);
  pre.step(3);
  CONV_SCHED(4);
  PIN4(a1);
  PINPRE(3);
  SCHED_FENCE();
  a1[0] = MFMA16(f84, b1_42, a1[0]);
  const u32x4 b1_44 = *(const u32x4*)(ub + (0 * UT_ROWS + 6) * 16);
  a1[1] = MFMA16(f85, b1_42, a1[1]);
  const u32x4 f90 = *(const u32x4*)(hb + 2880);
  a1[2] = MFMA16(f86, b1_42, a1[2]);
  const u32x4 f91 = *(const u32x4*)(hb + 2912);
  a1[3] = MFMA16(f87, b1_42, a1[3]);
  CONV_SCHED(4);
  PIN4(a1);
  SCHED_FENCE();
  a1[0] = MFMA16(f86, b1_43, a1[0]);
  const u32x4 b1_45 = *(const u32x4*)(ub + (4 * UT_ROWS + 6) * 16);
  a1[1] = MFMA16(f87, b1_43, a1[1]);
  const u32x4 f92 = *(const u32x4*)(hb + 2944);
  a1[2] = MFMA16(f88, b1_43, a1[2]);
  const u32x4 f93 = *(const u32x4*)(hb + 2976);
  a1[3] = MFMA16(f89, b1_43, a1[3]);
  pre.step(4);
  CONV_SCHED(4);
  PIN4(a1);
  PINPRE(4);
  SCHED_FENCE();
  a1[0] = MFMA16(f88, b1_44, a1[0]);
  const u32x4 b1_46 = *(const u32x4*)(ub + (0 * UT_ROWS + 7) * 16);
  a1[1] = MFMA16(f89, b1_44, a1[1]);
  const u32x4 f94 = *(const u32x4*)(hb + 3008);
  a1[2] = MFMA16(f90, b1_44, a1[2]);
  const u32x4 f95 = *(const u32x4*)(hb + 3040);
  a1[3] = MFMA16(f91, b1_44, a1[3]);
  pre.step(5);
  CONV_SCHED(4);
  PIN4(a1);
  PINPRE(5);
  SCHED_FENCE();
  a1[0] = MFMA16(f90, b1_45, a1[0]);
  const u32x4 b1_47 = *(const u32x4*)(ub + (4 * UT_ROWS + 7) * 16);
  a1[1] = MFMA16(f91, b1_45, a1[1]);
  const u32x4 f96 = *(const u32x4*)(hb + 3072);
  a1[2] = MFMA16(f92, b1_45, a1[2]);
  const u32x4 f97 = *(const u32x4*)(hb + 3104);
  a1[3] = MFMA16(f93, b1_45, a1[3]);
  CONV_SCHED(4);
  PIN4(a1);
  SCHED_FENCE();
  a1[0] = MFMA16(f92, b1_46, a1[0]);
  a1[1] = MFMA16(f93, b1_46, a1[1]);
  a1[2] = MFMA16(f94, b1_46, a1[2]);
  a1[3] = MFMA16(f95, b1_46, a1[3]);
  pre.step(6);
  CONV_SCHED(4);
  PIN4(a1);
  PINPRE(6);
  SCHED_FENCE();
  a1[0] = MFMA16(f94, b1_47, a1[0]);
  a1[1] = MFMA16(f95, b1_47, a1[1]);
  a1[2] = MFMA16(f96, b1_47, a1[2]);
  a1[3] = MFMA16(f97, b1_47, a1[3]);
  pre.step(7);
  CONV_SCHED(4);
  PIN4(a1);
  PINPRE(7);
  SCHED_FENCE();
}

extern "C" __global__ __launch_bounds__(768) void k1_conv(const u16* __restrict__ ssm_k, const u16* __restrict__ ssm_D,
                                               const u16* __restrict__ Wg, const u16* __restrict__ Wu,
                                               const u16* __restrict__ Wd, const float* __restrict__ modp,
                                               char* __restrict__ ws) {
  extern __shared__ __attribute__((aligned(16))) char smem[];
  const int t = threadIdx.x, b = blockIdx.x;
  const int lane = t & 63;
  const int wv = __builtin_amdgcn_readfirstlane(t >> 6);
  const int d0 = 64 * (b & 7) + 2 * (b >> 3);
  unsigned* cnt = (unsigned*)(smem + K1_OFF_M + 1024 + 32);

  if (wv >= 8) {
    const int tt = t - 512, wsd = wv - 8;
    __builtin_amdgcn_s_barrier();
    SCHED_FENCE();
    if (b >= 224) {
      if (b >= 254) {
        const int di = (b - 254) * 256 + tt;
        ((float*)(ws + WS_GB))[di] = mod_val(modp, 5 * 512 + di);
      }
      return;
    }
    const int fs = b >> 2, dq = b & 3;
    const int g = lane >> 4, mu = (lane >> 3) & 1, fq = lane & 7;
    const int kb = 4 * dq + wsd;
    float mp[8];
    {
      const int idx = ((lane >> 5) ? 3 : 4) * 512 + 32 * kb + (lane & 31);
#pragma unroll
      for (int p = 0; p < 8; ++p) mp[p] = modp[p * 3072 + idx];
    }
    u32x2 w1[8];
    unsigned wd[8];
    {
      const u16* W = (mu ? Wu : Wg) + (size_t)(32 * kb + 8 * g) * F_DIM + 32 * fs + 4 * fq;
#pragma unroll
      for (int i = 0; i < 8; ++i) w1[i] = *(const u32x2*)(W + i * F_DIM);
      const u16* W2 = Wd + (size_t)(32 * fs + 8 * g) * D_DIM + 128 * dq + 32 * wsd + 2 * (lane & 15);
#pragma unroll
      for (int i = 0; i < 8; ++i) wd[i] = *(const unsigned*)(W2 + i * D_DIM);
    }
    SCHED_FENCE();
    __builtin_amdgcn_s_setprio(1);
    float* mslot = (float*)(smem + K1_OFF_M) + wsd * 64;
    {
      float m = rbf(((mp[0] + mp[1]) + (mp[2] + mp[3])) + ((mp[4] + mp[5]) + (mp[6] + mp[7])));
      mslot[lane] = (lane >> 5) ? m : rbf(1.0f + m);
    }
    char* scr = wsd < 2 ? smem + K1_OFF_S + wsd * 2048 : smem + K1_OFF_Y + (wsd - 2) * Y_CH + 3 * Y_WAVE;
    {
      const f32x4* mq = (const f32x4*)mslot + 2 * g;
      f32x4 ca = mq[0], cb = mq[1], sa = mq[8], sb2 = mq[9];
      const float c1[8] = {ca[0], ca[1], ca[2], ca[3], cb[0], cb[1], cb[2], cb[3]};
      const float sb[8] = {sa[0], sa[1], sa[2], sa[3], sb2[0], sb2[1], sb2[2], sb2[3]};
      float bs[4];
      u32x4 pc[4];
#pragma unroll
      for (int e = 0; e < 4; ++e) {
        float x[8];
#pragma unroll
        for (int i = 0; i < 8; ++i) x[i] = (e & 1) ? bhi(w1[i][e >> 1]) : blo(w1[i][e >> 1]);
        float bsum = 0.f;
#pragma unroll
        for (int i = 0; i < 4; ++i) {
          pc[e][i] = pack2(x[2 * i] * c1[2 * i], x[2 * i + 1] * c1[2 * i + 1]);
          bsum = __builtin_fmaf(sb[2 * i], x[2 * i], __builtin_fmaf(sb[2 * i + 1], x[2 * i + 1], bsum));
        }
        bs[e] = bsum;
      }
      {
        auto s01 = __builtin_amdgcn_permlane16_swap(__builtin_bit_cast(unsigned, bs[0]), __builtin_bit_cast(unsigned, bs[1]), false, false);
        auto s23 = __builtin_amdgcn_permlane16_swap(__builtin_bit_cast(unsigned, bs[2]), __builtin_bit_cast(unsigned, bs[3]), false, false);
        const float p01 = __builtin_bit_cast(float, (unsigned)s01[0]) + __builtin_bit_cast(float, (unsigned)s01[1]);
        const float p23 = __builtin_bit_cast(float, (unsigned)s23[0]) + __builtin_bit_cast(float, (unsigned)s23[1]);
        auto tt2 = __builtin_amdgcn_permlane32_swap(__builtin_bit_cast(unsigned, p01), __builtin_bit_cast(unsigned, p23), false, false);
        const float tot = __builtin_bit_cast(float, (unsigned)tt2[0]) + __builtin_bit_cast(float, (unsigned)tt2[1]);
        const int R = (4 * fs + (fq >> 1)) * 16 + 8 * (fq & 1) + 4 * (g >> 1) + 2 * mu + (g & 1);
        *(float*)(ws + WS_CSB + ((size_t)kb * 3584 + R) * 8 + 4) = tot;
      }
      char* wpos = scr + ((fq >> 1) & 1) * 1024 + g * 256 + (8 * (fq & 1) + 2 * mu) * 16;
      char* dst = ws + WS_W1F + (size_t)(kb * 224 + 4 * fs) * 1024 + lane * 16;
      const u32x4 ones = {0x3f803f80u, 0x3f803f80u, 0x3f803f80u, 0x3f803f80u};
      const f32x4 z4 = {0.f, 0.f, 0.f, 0.f};
      f32x4 csv[4];
#pragma unroll
      for (int rho = 0; rho < 2; ++rho) {
        if ((fq >> 2) == rho) {
#pragma unroll
          for (int e = 0; e < 4; ++e) *(u32x4*)(wpos + (4 * (e >> 1) + (e & 1)) * 16) = pc[e];
        }
        u32x4 o0 = *(const u32x4*)(scr + lane * 16), o1 = *(const u32x4*)(scr + 1024 + lane * 16);
        st16_wt(dst + (2 * rho) * 1024, o0);
        st16_wt(dst + (2 * rho + 1) * 1024, o1);
        csv[2 * rho] = MFMA16(ones, o0, z4);
        csv[2 * rho + 1] = MFMA16(ones, o1, z4);
      }
      {
        const float cs = g == 0 ? csv[0][0] : g == 1 ? csv[1][0] : g == 2 ? csv[2][0] : csv[3][0];
        *(float*)(ws + WS_CSB + ((size_t)kb * 3584 + (4 * fs) * 16 + lane) * 8) = cs;
      }
      u32x4 q0, q1;
#pragma unroll
      for (int i = 0; i < 4; ++i) {
        q0[i] = __builtin_amdgcn_perm(wd[2 * i + 1], wd[2 * i], 0x05040100u);
        q1[i] = __builtin_amdgcn_perm(wd[2 * i + 1], wd[2 * i], 0x07060302u);
      }
      char* dpos = scr + ((lane >> 3) & 1) * 1024 + g * 256 + (lane & 7) * 32;
      *(u32x4*)dpos = q0;
      *(u32x4*)(dpos + 16) = q1;
      u32x4 o0 = *(const u32x4*)(scr + lane * 16), o1 = *(const u32x4*)(scr + 1024 + lane * 16);
      char* dw = ws + WS_WDF + (size_t)(fs * 32 + 8 * dq + 2 * wsd) * 1024 + lane * 16;
      st16_wt(dw, o0);
      st16_wt(dw + 1024, o1);
    }
    return;
  }

  const int hch = t >> 8, tau = t & 255;
  const u16* hrow = ssm_k + (size_t)(d0 + hch) * S_LEN;
  const int Q = tau - 1;
  const u32x4 zero4 = {0, 0, 0, 0};
  u32x4 h0 = *(const u32x4*)(hrow + 8 * (Q < 0 ? 0 : Q));
  u32x4 h1 = *(const u32x4*)(hrow + 8 * (Q + 1));
  const int ch = hch, idx = tau;
  const int uP = (tau & ~63) + (lane & 7) * 8 + (lane >> 3);
  u32x4 uu = *(const u32x4*)(ws + WS_KVT + ((size_t)(d0 + hch) * S_LEN + 8 * uP) * 2);
  if (t < 2) cnt[t] = 0u;
  LGKM0_BAR();
  ConvPre pre;
  pre.x = *(const u32x4*)(ws + WS_XT + ((size_t)(d0 + ch) * S_LEN + 8 * idx) * 2);
#pragma unroll
  for (int i = 0; i < 2; ++i) pre.st[i] = *(const u32x4*)(ws + WS_STAT1 + (size_t)idx * 32 + i * 16);
  SCHED_FENCE();
  pre.sp = modp[(lane >> 3) * 3072 + (lane & 3) * 512 + d0 + ch];
  pre.dD = ssm_D[d0 + ch];
  SCHED_FENCE();
  char* hb = smem + K1_OFF_H + hch * HC_BYTES;
  {
    const int z = tau & 63, c = (z >> 2) & 7;
    const int beta = (c & 1) + 4 * ((c >> 1) & 1) + 8 * (c >> 2);
    const int oa = K1_OFF_U + hch * UT_BYTES + ((tau >> 4) * UT_ROWS + 32 + (tau & 15)) * 16;
    const int ob = K1_OFF_V + hch * VT_BYTES + ((z >> 4) * 32 + 17 + ((z & 15) < 15 ? (z & 15) : 14)) * 16;
    const int oc = K1_OFF_H + hch * HC_BYTES + (c * HC_STRIDE + beta + 4 + ((z & 3) < 3 ? (z & 3) : 2)) * 16;
    *(u32x4*)(smem + (tau < 128 ? oa : tau < 192 ? ob : oc)) = zero4;
  }
  SCHED_FENCE();
  if (Q < 0) h0 = zero4;
  {
    unsigned w[8] = {h0[0], h0[1], h0[2], h0[3], h1[0], h1[1], h1[2], h1[3]};
    unsigned od[7];
#pragma unroll
    for (int i = 0; i < 7; ++i) od[i] = __builtin_amdgcn_alignbit(w[i + 1], w[i], 16);
#pragma unroll
    for (int c = 0; c < 8; ++c) {
      const int cp = c + 1, e = cp >> 1;
      const int beta = (c & 1) + 4 * ((c >> 1) & 1) + 8 * (c >> 2);
      u32x4 val;
      if (cp & 1) val = (u32x4){od[e], od[e + 1], od[e + 2], od[e + 3]};
      else val = (u32x4){w[e], w[e + 1], w[e + 2], w[e + 3]};
      *(u32x4*)(hb + (c * HC_STRIDE + beta + Q + 8) * 16) = val;
    }
    *(u32x4*)(smem + K1_OFF_U + hch * UT_BYTES + ((uP & 7) * UT_ROWS + (uP >> 3)) * 16) = uu;
    if (uP >= 188) *(u32x4*)(smem + K1_OFF_V + hch * VT_BYTES + ((uP & 3) * 32 + (uP >> 2) - 47) * 16) = uu;
  }
  conv_sync(cnt + hch, 4u, lane);
  {
    const int P = 255 - idx;
    pre.kv = *(const u32x4*)(smem + K1_OFF_U + ch * UT_BYTES + ((P & 7) * UT_ROWS + (P >> 3)) * 16);
  }
  f32x4 a0[4], a1[4], a3[2];
#pragma unroll
  for (int m = 0; m < 4; ++m) { a0[m] = (f32x4){0.f, 0.f, 0.f, 0.f}; a1[m] = (f32x4){0.f, 0.f, 0.f, 0.f}; }
  a3[0] = (f32x4){0.f, 0.f, 0.f, 0.f}; a3[1] = (f32x4){0.f, 0.f, 0.f, 0.f};
  const int wch = wv >> 2, wk = (wv + wch) & 3;
  const int c = lane & 15, gp = lane >> 4;
  {
    const int c8 = c & 7;
    const int beta = (c8 & 1) + 4 * ((c8 >> 1) & 1) + 8 * (c8 >> 2);
    const char* hbase = smem + K1_OFF_H + wch * HC_BYTES + (c8 * HC_STRIDE + beta + gp + (c >> 3)) * 16;
    const char* ubase = smem + K1_OFF_U + wch * UT_BYTES + (gp * UT_ROWS + 31 - c) * 16;
    const char* vbase = smem + K1_OFF_V + wch * VT_BYTES + (gp * 32 + 15 - c) * 16;
    const bool odd = c & 1;
    char* yb = smem + K1_OFF_Y + wch * Y_CH + wk * Y_WAVE + c * Y_ROW + gp * 16;
    char* y3 = smem + K1_OFF_Y + wch * Y_CH + (c >> 1) * Y_ROW + (c & 1) * 128 + gp * 16;
    if (wk == 0) {
      conv_w0(hbase, ubase, vbase, odd, a0, a1, a3, pre);
    } else if (wk == 1) {
      conv_w1(hbase, ubase, vbase, odd, a0, a1, a3, pre);
      y3 += 2 * Y_WAVE;
    } else if (wk == 2) {
      conv_w2(hbase, ubase, vbase, odd, a0, a1, a3, pre);
      y3 += 2 * Y_WAVE + 8 * Y_ROW;
    } else {
      conv_w3(hbase, ubase, vbase, odd, a0, a1, a3, pre);
      y3 += 3 * Y_WAVE + 8 * Y_ROW;
    }
#pragma unroll
    for (int mt = 0; mt < 4; ++mt) *(f32x4*)(yb + 16 * Y_ROW + mt * 64) = a1[mt];
    if (wk < 2) {
#pragma unroll
      for (int mt = 0; mt < 4; ++mt) *(f32x4*)(yb + mt * 64) = a0[mt];
    }
    if (wk > 0) { *(f32x4*)(y3) = a3[0]; *(f32x4*)(y3 + 64) = a3[1]; }
  }
  conv_sync(cnt + hch, 8u, lane);
  {
    const char* yb = smem + K1_OFF_Y + ch * Y_CH + (idx >> 3) * Y_ROW + (idx & 7) * 32;
    f32x4 y[2];
#pragma unroll
    for (int h = 0; h < 2; ++h) {
      y[h] = *(const f32x4*)(yb + 16 * h) + *(const f32x4*)(yb + 16 * h + Y_WAVE);
      if ((wv & 3) >= 1) y[h] += *(const f32x4*)(yb + 16 * h + 2 * Y_WAVE) + *(const f32x4*)(yb + 16 * h + 3 * Y_WAVE);
      if ((wv & 3) == 3) y[h] += *(const f32x4*)(yb + 16 * h + 2 * Y_WAVE - 24 * Y_ROW);
    }
    u32x4 o;
#pragma unroll
    for (int i = 0; i < 4; ++i) {
      o[i] = pack2(__builtin_fmaf(pre.h1[2 * i], y[i >> 1][2 * (i & 1)], pre.kd[2 * i]),
                   __builtin_fmaf(pre.h1[2 * i + 1], y[i >> 1][2 * (i & 1) + 1], pre.kd[2 * i + 1]));
    }
    st16_wt(ws + WS_X1T + ((size_t)(d0 + ch) * S_LEN + 8 * idx) * 2, o);
  }
}

#define G1_STAGE (22 * 1024)
#define G1_NST 7
#define G1_OFF_X (G1_NST * G1_STAGE)
#define G1_LDS (G1_OFF_X + 1792 + 1024)

#define GLDS16(gp, lp) __builtin_amdgcn_global_load_lds((const __attribute__((address_space(1))) void*)(gp), (__attribute__((address_space(3))) void*)(lp), 16, 0, 0)

__device__ __forceinline__ void g1_item(int k, const char* xsrc, const char* wsrc, const char* wmix, char* lds,
                                        char* ldsmix, int whi) {
  const int a = k / 11, r = k % 11;
  const int s0 = ((2 * a) % G1_NST) * G1_STAGE, s1 = ((2 * a + 1) % G1_NST) * G1_STAGE;
  if (r < 2) GLDS16(xsrc + (size_t)(2 * a) * (128 * 1024) + r * 65536, lds + s0 + r * 4096);
  else if (r < 5) GLDS16(wsrc + (size_t)(2 * a) * (224 * 1024) + (r - 2) * 4096, lds + s0 + 8192 + (r - 2) * 4096);
  else if (r == 5) GLDS16(wmix + (size_t)(2 * a) * (224 * 1024), ldsmix + s0 + whi * (s1 - s0));
  else if (r < 8) GLDS16(xsrc + (size_t)(2 * a + 1) * (128 * 1024) + (r - 6) * 65536, lds + s1 + (r - 6) * 4096);
  else GLDS16(wsrc + (size_t)(2 * a + 1) * (224 * 1024) + (r - 8) * 4096, lds + s1 + 8192 + (r - 8) * 4096);
}
template <int N> __device__ __forceinline__ void wait_vm() { asm volatile("s_waitcnt vmcnt(%0)" ::"i"(N) : "memory"); }
__device__ __forceinline__ void g2_item(int j, int w, const char* wdsrc, char* smem) {
  const int i = j >> 2, kq = j & 3;
  const char* src = wdsrc + (size_t)(14 * kq + 5 + i) * (32 * 1024);
  if (j < 27) GLDS16(src, smem + (44 + 4 * j) * 1024 + w * 1024);
  else if (j > 27) GLDS16(src, smem + (4 * j - 110) * 1024 + w * 1024);
  else GLDS16(src, smem + (w < 2 ? 152 + w : w - 2) * 1024);
}
template <int NM>
__device__ __forceinline__ void g1_mma(f32x4 (&acc)[4][4], const u32x4 (&xf)[4], const u32x4 (&wf)[NM], int half) {
#pragma unroll
  for (int m = 0; m < NM; ++m)
#pragma unroll
    for (int n = 0; n < 4; ++n) acc[m][n] = MFMA16(wf[m], xf[n], acc[m][n]);
  if constexpr (NM == 3) {
    const u32x4 ones = {0x3f803f80u, 0x3f803f80u, 0x3f803f80u, 0x3f803f80u};
    acc[3][0] = MFMA16(xf[0], xf[0], acc[3][0]);
    acc[3][1] = MFMA16(xf[1], xf[1], acc[3][1]);
    acc[3][2] = MFMA16(ones, xf[0], acc[3][2]);
    acc[3][3] = MFMA16(ones, xf[1], acc[3][3]);
  }
#pragma unroll
  for (int _i = 0; _i < 8 + NM; ++_i) {
    __builtin_amdgcn_sched_group_barrier(0x008, 1, 0);
    __builtin_amdgcn_sched_group_barrier(0x100, 1, 0);
  }
  __builtin_amdgcn_sched_group_barrier(0x008, 16, 0);
#pragma unroll
  for (int m = 0; m < 4; ++m) asm volatile("" : "+v"(acc[m][0]), "+v"(acc[m][1]), "+v"(acc[m][2]), "+v"(acc[m][3]));
}

template <int NM>
__device__ __forceinline__ void g1_compute(char* __restrict__ ws, char* smem, int lane, int ws_, int half, int rt, int ct) {
  f32x2* sst = (f32x2*)(smem + G1_OFF_X + 1792);
  const int m0 = 7 * half + (NM == 3 ? 4 : 0);
  f32x4 acc[4][4];
#pragma unroll
  for (int m = 0; m < 4; ++m)
#pragma unroll
    for (int n = 0; n < 4; ++n) acc[m][n] = (f32x4){0.f, 0.f, 0.f, 0.f};
  const int xr = (NM == 3) ? 2 * half : 0;
  const char* xl[4];
  {
    const int g = lane >> 4, qq = (lane & 15) >> 2, pp = lane & 3;
    const int phi = qq | ((g & 1) << 2);
#pragma unroll
    for (int n = 0; n < 4; ++n) xl[n] = smem + 256 * (8 * g + qq) + 32 * ((4 * ws_ + (n ^ xr)) ^ phi) + 8 * pp;
  }
  const char* wl = smem + (8 + m0) * 1024 + lane * 16;
  u32x4 xa[4], wa[NM], xb[4], wb[NM];
#define G1_READ(X, W, so) do { _Pragma("unroll") for (int n = 0; n < 4; ++n) { \
      u32x2 lo_ = lds_tr(xl[n] + (so)), hi_ = lds_tr(xl[n] + (so) + 1024); X[n] = (u32x4){lo_[0], lo_[1], hi_[0], hi_[1]}; } \
    _Pragma("unroll") for (int m = 0; m < NM; ++m) W[m] = *(const u32x4*)(wl + (so) + m * 1024); } while (0)
  LGKM0_BAR();
  G1_READ(xa, wa, 0);
  int so = G1_STAGE;
#pragma unroll 1
  for (int it = 0; it < 7; ++it) {
    LGKM0_BAR();
    G1_READ(xb, wb, so);
    g1_mma<NM>(acc, xa, wa, half);
    so += G1_STAGE; if (so == G1_NST * G1_STAGE) so = 0;
    LGKM0_BAR();
    G1_READ(xa, wa, so);
    g1_mma<NM>(acc, xb, wb, half);
    so += G1_STAGE; if (so == G1_NST * G1_STAGE) so = 0;
  }
  LGKM0_BAR();
  G1_READ(xb, wb, so);
  g1_mma<NM>(acc, xa, wa, half);
  const int c = lane & 15, gp = lane >> 4;
  f32x4 cb[NM][2];
#pragma unroll
  for (int m = 0; m < NM; ++m) {
    const f32x4* cp = (const f32x4*)(smem + G1_OFF_X + ((m0 + m) * 4 + gp) * 32);
    cb[m][0] = cp[0]; cb[m][1] = cp[1];
  }
  if constexpr (NM == 3) {
    const u32x4 ones = {0x3f803f80u, 0x3f803f80u, 0x3f803f80u, 0x3f803f80u};
    acc[3][0] = MFMA16(xb[0], xb[0], acc[3][0]);
    acc[3][1] = MFMA16(xb[1], xb[1], acc[3][1]);
    acc[3][2] = MFMA16(ones, xb[0], acc[3][2]);
    acc[3][3] = MFMA16(ones, xb[1], acc[3][3]);
  }
  if constexpr (NM == 3) {
#pragma unroll
    for (int n = 0; n < 2; ++n) {
      float ss = (c & 3) == 0 ? acc[3][n][0] : (c & 3) == 1 ? acc[3][n][1] : (c & 3) == 2 ? acc[3][n][2] : acc[3][n][3];
      float mean = acc[3][2 + n][0] * (1.0f / D_DIM);
      float var = ss * (1.0f / D_DIM) - mean * mean;
      var = rbf(fmaxf(var, 0.f));
      float sd = rbf(__builtin_amdgcn_sqrtf(rbf(var + 1e-5f)));
      float rstd = __builtin_amdgcn_rcpf(sd);
      if (gp == (c >> 2)) sst[(4 * ws_ + xr + n) * 16 + c] = (f32x2){rstd, -rbf(mean) * rstd};
    }
  }
  LGKM0_BAR();
  {
    f32x2 stt[4];
#pragma unroll
    for (int n = 0; n < 4; ++n) stt[n] = sst[(4 * ws_ + (n ^ xr)) * 16 + c];
    char* actf = ws + WS_ACTF + ((size_t)((2 * rt + ws_) * 224 + (gp ^ xr)) * 64 + c) * 16;
    unsigned res[NM][4];
#pragma unroll
    for (int n = 0; n < 4; ++n) acc[0][n] = MFMA16(wb[0], xb[n], acc[0][n]);
#pragma unroll
    for (int m = 0; m < NM; ++m) {
      if (m + 1 < NM) {
#pragma unroll
        for (int n = 0; n < 4; ++n) acc[(m + 1) % NM][n] = MFMA16(wb[(m + 1) % NM], xb[n], acc[(m + 1) % NM][n]);
      }
      const f32x2 csg = {cb[m][0][0], cb[m][0][1]}, bbg = {cb[m][0][2], cb[m][0][3]};
      const f32x2 csu = {cb[m][1][0], cb[m][1][1]}, bbu = {cb[m][1][2], cb[m][1][3]};
      f32x2 g[4], u[4];
#pragma unroll
      for (int n = 0; n < 4; ++n) {
        const f32x2 b2 = {stt[n][1], stt[n][1]};
        g[n] = __builtin_elementwise_fma(b2, csg, bbg);
        u[n] = __builtin_elementwise_fma(b2, csu, bbu);
      }
#pragma unroll
      for (int n = 0; n < 4; ++n) {
        const f32x2 a2 = {stt[n][0], stt[n][0]};
        g[n] = __builtin_elementwise_fma(a2, (f32x2){acc[m][n][0], acc[m][n][1]}, g[n]);
        u[n] = __builtin_elementwise_fma(a2, (f32x2){acc[m][n][2], acc[m][n][3]}, u[n]);
      }
#pragma unroll
      for (int n = 0; n < 4; ++n) {
        g[n][0] = fmaxf(g[n][0], 0.f); g[n][1] = fmaxf(g[n][1], 0.f);
        f32x2 a = g[n] * u[n];
        res[m][n] = pack2(a[0], a[1]);
      }
      auto s01 = __builtin_amdgcn_permlane16_swap(res[m][0], res[m][1], false, false);
      auto s23 = __builtin_amdgcn_permlane16_swap(res[m][2], res[m][3], false, false);
      auto t02 = __builtin_amdgcn_permlane32_swap(s01[0], s23[0], false, false);
      auto t13 = __builtin_amdgcn_permlane32_swap(s01[1], s23[1], false, false);
      const int mt = 14 * ct + m0 + m;
      st16_wt(actf + ((mt >> 2) * 4 * 64 + (mt & 3) * 16) * 16, (u32x4){t02[0], t13[0], t02[1], t13[1]});
      SCHED_FENCE();
    }
  }
}

#define G2_OFF_XT (34 * 1024)

extern "C" __global__ __launch_bounds__(768, 3) void k2_mlp(char* __restrict__ ws, u16* __restrict__ out) {
  extern __shared__ __attribute__((aligned(16))) char smem[];
  const int t = threadIdx.x, b = blockIdx.x;
  const int lane = t & 63;
  int wv = __builtin_amdgcn_readfirstlane(t >> 6);
  const int xcd = b & 7, idx = b >> 3;
  const int rt = 4 * (xcd >> 1) + (idx >> 3), ct = 8 * (xcd & 1) + (idx & 7);
  const int B = 2 * rt + (ct >> 3), dtile = ct & 7;
  unsigned* cnt = (unsigned*)(ws + WS_CNT + rt * 256);

#define G1_ITEM(k) g1_item(k, xsrc, wsrc, wmix, lds, ldsmix, whi)
  if (wv >= 8) {
    __builtin_amdgcn_s_setprio(3);
    const int w = wv - 8;
    const int whi = w >> 1;
    const int xr_ = lane >> 4, xphi = xr_ | (whi << 2);
    const char* xsrc = ws + WS_X1T + (size_t)(4 * w + xr_) * 4096 + 256 * rt + (((lane & 15) ^ (xphi << 1)) * 16);
    const char* wsrc = ws + WS_W1F + (size_t)(14 * ct + w) * 1024 + lane * 16;
    const char* wmix = ws + WS_W1F + (size_t)(224 * whi + 14 * ct + 12 + (w & 1)) * 1024 + lane * 16;
    char* lds = smem + w * 1024;
    char* ldsmix = smem + (20 + (w & 1)) * 1024;
#pragma unroll
    for (int k = 0; k < 11; ++k) G1_ITEM(k);
  }
  SCHED_FENCE();
  asm volatile("" : "+s"(wv));
  SCHED_FENCE();
  if (wv >= 8) {
    const int w = wv - 8;
    const int whi = w >> 1;
    const int xr_ = lane >> 4, xphi = xr_ | (whi << 2);
    const char* xsrc = ws + WS_X1T + (size_t)(4 * w + xr_) * 4096 + 256 * rt + (((lane & 15) ^ (xphi << 1)) * 16);
    const char* wsrc = ws + WS_W1F + (size_t)(14 * ct + w) * 1024 + lane * 16;
    const char* wmix = ws + WS_W1F + (size_t)(224 * whi + 14 * ct + 12 + (w & 1)) * 1024 + lane * 16;
    char* lds = smem + w * 1024;
    char* ldsmix = smem + (20 + (w & 1)) * 1024;
    const char* wdsrc = ws + WS_WDF + (size_t)(4 * dtile + w) * 1024 + lane * 16;
    const int R = 56 * w + (lane < 56 ? lane : 55);
    const f32x2* csbp = (const f32x2*)(ws + WS_CSB) + 224 * ct + R;
    float* csbd = (float*)(smem + G1_OFF_X) + (R >> 2) * 8 + ((R >> 1) & 1) * 4 + (R & 1);
    const char* wrsrc = ws + WS_WDF + ((size_t)(14 * w) * 32 + 4 * dtile) * 1024 + lane * 16;
    f32x2 pp[16];
    u32x4 wr[20];
#define G2_ITEM(j) g2_item(j, w, wdsrc, smem)
#define G2_RLOAD(r) asm volatile("global_load_dwordx4 %0, %1, off" : "=v"(wr[r]) : "v"(wrsrc + (size_t)((r) >> 2) * 32768 + ((r) & 3) * 1024) : "memory")
#define CSB_LOAD(i) asm volatile("global_load_dwordx2 %0, %1, off" : "=v"(pp[i]) : "v"(csbp + (i) * 3584) : "memory")
#define HBAR() do { SCHED_FENCE(); MEM_FENCE(); __builtin_amdgcn_s_barrier(); MEM_FENCE(); SCHED_FENCE(); } while (0)
#define CSB_FINISH() do { \
    _Pragma("unroll") for (int i = 0; i < 16; i += 4) asm volatile("" : "+v"(pp[i]), "+v"(pp[i + 1]), "+v"(pp[i + 2]), "+v"(pp[i + 3])); \
    f32x2 s_ = (((pp[0] + pp[1]) + (pp[2] + pp[3])) + ((pp[4] + pp[5]) + (pp[6] + pp[7]))) + \
               (((pp[8] + pp[9]) + (pp[10] + pp[11])) + ((pp[12] + pp[13]) + (pp[14] + pp[15]))); \
    csbd[0] = s_[0]; csbd[2] = s_[1]; \
    asm volatile("s_waitcnt lgkmcnt(0)" ::: "memory"); } while (0)
    wait_vm<5>();
    HBAR();
    G1_ITEM(11);
    G1_ITEM(12);
    G1_ITEM(13);
    G1_ITEM(14);
    G1_ITEM(15);
    G1_ITEM(16);
    G1_ITEM(17);
    G1_ITEM(18);
    wait_vm<8>();
    HBAR();
    G1_ITEM(19);
    G1_ITEM(20);
    G1_ITEM(21);
    G1_ITEM(22);
    G1_ITEM(23);
    G1_ITEM(24);
    G1_ITEM(25);
    G1_ITEM(26);
    wait_vm<10>();
    HBAR();
    G1_ITEM(27);
    G1_ITEM(28);
    G1_ITEM(29);
    G1_ITEM(30);
    G1_ITEM(31);
    G1_ITEM(32);
    G1_ITEM(33);
    G1_ITEM(34);
    wait_vm<13>();
    HBAR();
    G1_ITEM(35);
    G1_ITEM(36);
    G1_ITEM(37);
    G1_ITEM(38);
    G1_ITEM(39);
    G1_ITEM(40);
    G2_RLOAD(0);
    G2_RLOAD(1);
    wait_vm<15>();
    HBAR();
    G1_ITEM(41);
    G1_ITEM(42);
    G1_ITEM(43);
    G1_ITEM(44);
    G1_ITEM(45);
    G2_RLOAD(2);
    G2_RLOAD(3);
    G2_RLOAD(4);
    wait_vm<18>();
    HBAR();
    G1_ITEM(46);
    G1_ITEM(47);
    G1_ITEM(48);
    G1_ITEM(49);
    G1_ITEM(50);
    G1_ITEM(51);
    G2_RLOAD(5);
    G2_RLOAD(6);
    wait_vm<20>();
    HBAR();
    G1_ITEM(52);
    G1_ITEM(53);
    G1_ITEM(54);
    G1_ITEM(55);
    G1_ITEM(56);
    G2_RLOAD(7);
    G2_RLOAD(8);
    G2_RLOAD(9);
    wait_vm<21>();
    HBAR();
    G1_ITEM(57);
    G1_ITEM(58);
    G1_ITEM(59);
    G1_ITEM(60);
    G1_ITEM(61);
    G1_ITEM(62);
    G2_RLOAD(10);
    G2_RLOAD(11);
    wait_vm<20>();
    HBAR();
    G1_ITEM(63);
    G1_ITEM(64);
    G1_ITEM(65);
    G1_ITEM(66);
    G1_ITEM(67);
    CSB_LOAD(0);
    CSB_LOAD(1);
    CSB_LOAD(2);
    CSB_LOAD(3);
    G2_RLOAD(12);
    G2_RLOAD(13);
    wait_vm<24>();
    HBAR();
    G1_ITEM(68);
    G1_ITEM(69);
    G1_ITEM(70);
    G1_ITEM(71);
    G1_ITEM(72);
    G1_ITEM(73);
    CSB_LOAD(4);
    CSB_LOAD(5);
    CSB_LOAD(6);
    CSB_LOAD(7);
    G2_RLOAD(14);
    wait_vm<26>();
    HBAR();
    G1_ITEM(74);
    G1_ITEM(75);
    G1_ITEM(76);
    G1_ITEM(77);
    G1_ITEM(78);
    CSB_LOAD(8);
    CSB_LOAD(9);
    CSB_LOAD(10);
    CSB_LOAD(11);
    G2_ITEM(0);
    G2_ITEM(1);
    wait_vm<30>();
    HBAR();
    G1_ITEM(79);
    G1_ITEM(80);
    G1_ITEM(81);
    G1_ITEM(82);
    G1_ITEM(83);
    G1_ITEM(84);
    CSB_LOAD(12);
    CSB_LOAD(13);
    CSB_LOAD(14);
    CSB_LOAD(15);
    G2_ITEM(2);
    wait_vm<29>();
    HBAR();
    G1_ITEM(85);
    G1_ITEM(86);
    G1_ITEM(87);
    G2_ITEM(3);
    G2_ITEM(4);
    G2_ITEM(5);
    G2_ITEM(6);
    G2_ITEM(7);
    wait_vm<27>();
    HBAR();
    G2_ITEM(8);
    G2_ITEM(9);
    G2_ITEM(10);
    G2_ITEM(11);
    G2_ITEM(12);
    G2_ITEM(13);
    G2_ITEM(14);
    G2_ITEM(15);
    wait_vm<23>();
    HBAR();
    G2_ITEM(16);
    G2_ITEM(17);
    G2_ITEM(18);
    G2_ITEM(19);
    G2_ITEM(20);
    G2_ITEM(21);
    G2_ITEM(22);
    G2_ITEM(23);
    wait_vm<21>();
    CSB_FINISH();
    HBAR();
    G2_ITEM(24);
    G2_ITEM(25);
    G2_ITEM(26);
    G2_ITEM(27);
    G2_ITEM(28);
    G2_ITEM(29);
    G2_ITEM(30);
    G2_ITEM(31);
    HBAR();
    G2_ITEM(32);
    G2_ITEM(33);
    G2_ITEM(34);
    G2_ITEM(35);
    G2_RLOAD(15);
    G2_RLOAD(16);
    G2_RLOAD(17);
    G2_RLOAD(18);
    G2_RLOAD(19);
    wait_vm<0>();
    HBAR();
    if (w == 0) {
      if (lane == 0) __hip_atomic_fetch_add(cnt, 1u, __ATOMIC_RELAXED, __HIP_MEMORY_SCOPE_AGENT);
      unsigned spins = 0;
      while (__builtin_amdgcn_readfirstlane(__hip_atomic_load(cnt, __ATOMIC_RELAXED, __HIP_MEMORY_SCOPE_AGENT)) < 16u &&
             ++spins < (1u << 20))
        ;
      HBAR(); HBAR();
    }
#pragma unroll
    for (int h = 0; h < 2; ++h)
      GLDS16(ws + WS_X1T + ((size_t)(64 * dtile + 16 * w + 8 * h + (lane >> 3)) * S_LEN + 64 * B + 8 * (lane & 7)) * 2,
             smem + G2_OFF_XT + (2 * w + h) * 1024);
    if (w != 0) { HBAR(); HBAR(); }
    wait_vm<0>();
#pragma unroll
    for (int r = 0; r < 20; r += 4) asm volatile("" : "+v"(wr[r]), "+v"(wr[r + 1]), "+v"(wr[r + 2]), "+v"(wr[r + 3]));
    HBAR();
#pragma unroll
    for (int r = 0; r < 20; ++r) *(u32x4*)(smem + (44 + 16 * (r >> 2) + 4 * w + (r & 3)) * 1024 + lane * 16) = wr[r];
    LGKM0_BAR();
    return;
  }
  __builtin_amdgcn_s_waitcnt(0x0F70);
  {
    const int ws_ = (wv & 3) >> 1, half = wv & 1;
    if (wv < 4) g1_compute<4>(ws, smem, lane, ws_, half, rt, ct);
    else g1_compute<3>(ws, smem, lane, ws_, half, rt, ct);
  }
  wait_vm<0>();
  __builtin_amdgcn_s_waitcnt(0x0F70);
#define CBAR() HBAR()
  CBAR();

  const int kq = wv & 3, nh = wv >> 2;
  const int c = lane & 15, gp = lane >> 4;
  const f32x4 gb = *(const f32x4*)(ws + WS_GB + (size_t)(64 * dtile + 16 * kq + 4 * gp) * 4);
  f32x4 acc[4][2];
#pragma unroll
  for (int m = 0; m < 4; ++m) { acc[m][0] = (f32x4){0.f, 0.f, 0.f, 0.f}; acc[m][1] = (f32x4){0.f, 0.f, 0.f, 0.f}; }
  const char* wl[4];
  int w6[4];
#pragma unroll
  for (int j = 0; j < 4; ++j) {
    const int l4 = 4 * kq + ((kq + j) & 3);
    wl[j] = smem + lane * 16 + l4 * 1024;
    w6[j] = (l4 < 14 ? 140 : -14) * 1024;
  }
#define G2_WREAD(dst, blk) do { _Pragma("unroll") for (int m = 0; m < 4; ++m) { \
      if ((blk) < 6) dst[m] = *(const u32x4*)(wl[m] + (44 + 16 * (blk)) * 1024); \
      else if ((blk) > 6) dst[m] = *(const u32x4*)(wl[m] + (16 * (blk) - 110) * 1024); \
      else dst[m] = *(const u32x4*)(wl[m] + w6[m]); } } while (0)
  u32x4 wn[4];
  G2_WREAD(wn, 0);
  CBAR();
  {
    const __amdgpu_buffer_rsrc_t ra = __builtin_amdgcn_make_buffer_rsrc(ws + WS_ACTF, 0, 7340032, 0x00020000);
    const int voff = lane * 16;
    const int sbase = ((B * 56 + 14 * kq) * 4 + 2 * nh) * 1024;
    u32x4 af[14][2];
#define G2_LOAD(e) do { const int ks_ = (e) < 9 ? 5 + (e) : (e) - 9; \
      af[e][0] = __builtin_amdgcn_raw_buffer_load_b128(ra, voff, sbase + ks_ * 4096, 16); \
      af[e][1] = __builtin_amdgcn_raw_buffer_load_b128(ra, voff + 1024, sbase + ks_ * 4096, 16); } while (0)
#define G2_DEPTH 4
    G2_LOAD(0); G2_LOAD(1); CBAR();
    G2_LOAD(2); G2_LOAD(3);
    SCHED_FENCE();
#pragma unroll
    for (int e = 0; e < 14; ++e) {
      u32x4 wc[4];
#pragma unroll
      for (int m = 0; m < 4; ++m) wc[m] = wn[m];
      if (e == 4) LGKM0_BAR();
      if (e == 8) LGKM0_BAR();
      if (e < 13) G2_WREAD(wn, (e + 1 < 9 ? e + 1 : e + 1 - 9));
#pragma unroll
      for (int m = 0; m < 4; ++m) {
        acc[m][0] = MFMA16(wc[m], af[e][0], acc[m][0]);
        acc[m][1] = MFMA16(wc[m], af[e][1], acc[m][1]);
      }
#pragma unroll
      for (int m = 0; m < 4; m += 2) asm volatile("" : "+v"(acc[m][0]), "+v"(acc[m][1]), "+v"(acc[m + 1][0]), "+v"(acc[m + 1][1]));
      SCHED_FENCE();
      if (e + G2_DEPTH < 14) G2_LOAD((e + G2_DEPTH < 14 ? e + G2_DEPTH : 0));
      SCHED_FENCE();
    }
  }
#define G2_RED(r) (smem + ((r) < 15 ? 124 + 2 * (r) : 2 * ((r) - 15)) * 1024 + lane * 16)
#pragma unroll
  for (int j = 1; j < 4; ++j) {
    char* dst = G2_RED((nh * 4 + ((kq + j) & 3)) * 3 + j - 1);
    *(f32x4*)dst = acc[j][0];
    *(f32x4*)(dst + 1024) = acc[j][1];
  }
  f32x4 y[2] = {acc[0][0], acc[0][1]};
  u32x2 xv[2];
  {
    const int qq = c >> 2, pp = lane & 3;
#pragma unroll
    for (int n = 0; n < 2; ++n) xv[n] = lds_tr(smem + G2_OFF_XT + (16 * kq + 4 * gp + qq) * 128 + (32 * nh + 16 * n + 4 * pp) * 2);
  }
  const char* r0 = G2_RED((nh * 4 + kq) * 3), *r1 = G2_RED((nh * 4 + kq) * 3 + 1), *r2 = G2_RED((nh * 4 + kq) * 3 + 2);
  u16* dst = out + (size_t)(64 * B + 32 * nh + 16 * (gp & 1) + c) * D_DIM + 64 * dtile + 16 * kq + 8 * (gp >> 1);
  LGKM0_BAR();
  {
    unsigned o[2][2];
#pragma unroll
    for (int n = 0; n < 2; ++n) {
      y[n] += (*(const f32x4*)(r0 + n * 1024) + *(const f32x4*)(r1 + n * 1024)) + *(const f32x4*)(r2 + n * 1024);
      f32x2 ra = __builtin_elementwise_fma((f32x2){y[n][0], y[n][1]}, (f32x2){gb[0], gb[1]}, unpk(xv[n][0]));
      f32x2 rb = __builtin_elementwise_fma((f32x2){y[n][2], y[n][3]}, (f32x2){gb[2], gb[3]}, unpk(xv[n][1]));
      o[n][0] = pack2(ra[0], ra[1]);
      o[n][1] = pack2(rb[0], rb[1]);
    }
    auto s0 = __builtin_amdgcn_permlane16_swap(o[0][0], o[1][0], false, false);
    auto s1 = __builtin_amdgcn_permlane16_swap(o[0][1], o[1][1], false, false);
    *(u32x4*)dst = (u32x4){s0[0], s1[0], s0[1], s1[1]};
  }
}

extern "C" void kernel_launch(void* const* d_in, const int* in_sizes, int n_in, void* d_out, int out_size,
                              void* d_ws, size_t ws_size, hipStream_t stream) {
  const u16* q = (const u16*)d_in[0];
  const u16* k = (const u16*)d_in[1];
  const u16* v = (const u16*)d_in[2];
  const u16* shift_k = (const u16*)d_in[3];
  const u16* ssm_k = (const u16*)d_in[4];
  const u16* ssm_D = (const u16*)d_in[5];
  const u16* Wg = (const u16*)d_in[7];
  const u16* Wu = (const u16*)d_in[8];
  const u16* Wd = (const u16*)d_in[9];
  const u16* cvec = (const u16*)d_in[10];
  const u16* w_mod = (const u16*)d_in[11];
  char* ws = (char*)d_ws;
  (void)hipFuncSetAttribute((const void*)k1_conv, hipFuncAttributeMaxDynamicSharedMemorySize, K1_LDS);
  (void)hipFuncSetAttribute((const void*)k2_mlp, hipFuncAttributeMaxDynamicSharedMemorySize, G1_LDS);
  k0_prep<<<256, 896, 0, stream>>>(q, k, v, shift_k, cvec, w_mod, ws);
  k1_conv<<<256, 768, K1_LDS, stream>>>(ssm_k, ssm_D, Wg, Wu, Wd, (const float*)(ws + WS_MODP), ws);
  k2_mlp<<<256, 768, G1_LDS, stream>>>(ws, (u16*)d_out);
}
```
